```python
import torch

def _softmax(x, axis=-1):
    return torch.softmax(x, dim=axis)
def _gelu(x):
    return torch.nn.functional.gelu(x, approximate="tanh")
def _silu(x):
    return torch.nn.functional.silu(x)
def _sigmoid(x):
    return torch.sigmoid(x)
def _layernorm(x, axis=-1, eps=1e-5):
    m = x.mean(dim=axis, keepdim=True)
    v = x.var(dim=axis, keepdim=True, unbiased=False)
    return (x - m) / torch.sqrt(v + eps)
def _scatter_add(tgt, idx, src, axis=0):
    return tgt.clone().index_add_(axis, idx, src)
def _segment_sum(src, seg, n):
    out = torch.zeros((n,) + src.shape[1:], dtype=src.dtype)
    return out.index_add_(0, seg, src)
def _pad(x, pw):
    flat = tuple(v for pair in reversed(pw) for v in pair)
    return torch.nn.functional.pad(x, flat)

kernel_name = 'parallel_sum__mha_block__swiglu_ffn__prompt'

def setup_inputs():

    _w = (lambda a: (lambda t: t.to(torch.bfloat16) if t.is_floating_point() else t)(torch.as_tensor(a)))
    def _A_setup():
        B, S, D, H = 1, 2048, 512, 32
        return {
            "x": _w((lambda *s: torch.randn(*s, dtype=torch.bfloat16) * 0.05)(B, S, D)),
            "wqkv": _w((lambda *s: torch.randn(*s, dtype=torch.bfloat16) * 0.2)(D, 3 * D)),
            "wo": _w((lambda *s: torch.randn(*s, dtype=torch.bfloat16) * 0.1)(D, D)),
        }
    _A_in = _A_setup()

    def _B_setup():
        B, S, D, F = 1, 2048, 512, 1376
        return {
            "x": _w((lambda *s: torch.randn(*s, dtype=torch.bfloat16) * 0.05)(B, S, D)),
            "w_gate": _w((lambda *s: torch.randn(*s, dtype=torch.bfloat16) * 0.1)(D, F)),
            "w_up": _w((lambda *s: torch.randn(*s, dtype=torch.bfloat16) * 0.1)(D, F)),
            "w_down": _w((lambda *s: torch.randn(*s, dtype=torch.bfloat16) * 0.1)(F, D)),
        }
    _B_in = _B_setup()

    return {
        "A_x": _A_in["x"],
        "A_wqkv": _A_in["wqkv"],
        "A_wo": _A_in["wo"],
        "B_x": _B_in["x"],
        "B_w_gate": _B_in["w_gate"],
        "B_w_up": _B_in["w_up"],
        "B_w_down": _B_in["w_down"],
    }

def reference(A_x, A_wqkv, A_wo, B_x, B_w_gate, B_w_up, B_w_down):
    def _A_ref(x, wqkv, wo):
        B, S, D = x.shape
        H = 32
        Dh = D // H
        qkv = (lambda x, *d: x.permute(*d))((x @ wqkv).reshape(B, S, 3, H, Dh), 2, 0, 3, 1, 4)
        q, k, v = qkv[0], qkv[1], qkv[2]
        scores = torch.einsum("bhqd,bhkd->bhqk", q, k) * (Dh ** -0.5)
        p = _softmax(scores, axis=-1)
        o = torch.einsum("bhqk,bhkd->bhqd", p, v)
        return (lambda x, *d: x.permute(*d))(o, 0, 2, 1, 3).reshape(B, S, D) @ wo

    def _B_ref(x, w_gate, w_up, w_down):
        g = _silu(x @ w_gate)
        u = x @ w_up
        return (g * u) @ w_down

    x = A_x
    xn = _layernorm(x, axis=-1)
    return x + _A_ref(xn, A_wqkv, A_wo) + _B_ref(xn, B_w_gate, B_w_up, B_w_down)
```

```cpp
#include <hip/hip_runtime.h>
#include <hip/hip_bf16.h>

typedef short bf16x8 __attribute__((ext_vector_type(8)));
typedef short s16x4 __attribute__((ext_vector_type(4)));
typedef float f32x16 __attribute__((ext_vector_type(16)));
typedef float f32x4 __attribute__((ext_vector_type(4)));
typedef unsigned u32x4 __attribute__((ext_vector_type(4)));
typedef unsigned u32x2 __attribute__((ext_vector_type(2)));
typedef unsigned short u16;
typedef __attribute__((address_space(3))) const char* lds_cptr;

#define DI __device__ __forceinline__
#define MFMA(a, b, c) __builtin_amdgcn_mfma_f32_32x32x16_bf16((a), (b), (c), 0, 0, 0)

constexpr int S_ = 2048, D_ = 512, F_ = 1376, NH = 32;
constexpr int KST3 = 118;
constexpr size_t WS_Q = 0;
constexpr size_t WS_K = WS_Q + (size_t)NH * 64 * 1024;
constexpr size_t WS_V = WS_K + (size_t)NH * 64 * 1024;
constexpr size_t WS_A = WS_V + (size_t)NH * 64 * 1024;
constexpr size_t WS_W = WS_A + (size_t)64 * KST3 * 1024;

DI unsigned cvtpk(float lo, float hi) { unsigned r; asm("v_cvt_pk_bf16_f32 %0, %1, %2" : "=v"(r) : "v"(lo), "v"(hi)); return r; }
DI float dot2(unsigned a, unsigned b, float c) { float r; asm("v_dot2_f32_bf16 %0, %1, %2, %3" : "=v"(r) : "v"(a), "v"(b), "v"(c)); return r; }
DI float swap_add(float v) {
  auto rr = __builtin_amdgcn_permlane32_swap(__float_as_uint(v), __float_as_uint(v), false, false);
  return __uint_as_float(rr[0]) + __uint_as_float(rr[1]);
}
DI float swap_max(float v) {
  auto rr = __builtin_amdgcn_permlane32_swap(__float_as_uint(v), __float_as_uint(v), false, false);
  return __builtin_fmaxf(__uint_as_float(rr[0]), __uint_as_float(rr[1]));
}
DI s16x4 tr_read(lds_cptr p) {
  typedef short v4i16_t __attribute__((ext_vector_type(4)));
  return __builtin_bit_cast(s16x4, __builtin_amdgcn_ds_read_tr16_b64_v4i16((__attribute__((address_space(3))) v4i16_t*)p));
}
DI bf16x8 lds_read16(lds_cptr p) { return *(const __attribute__((address_space(3))) bf16x8*)p; }
DI void glds16(const void* g, char* lds) {
  __builtin_amdgcn_global_load_lds((const __attribute__((address_space(1))) void*)g, (__attribute__((address_space(3))) void*)lds, 16, 0, 0);
}
#define WAITV(n) asm volatile("s_waitcnt vmcnt(" #n ")" ::: "memory")
#define BAR() __builtin_amdgcn_s_barrier()

constexpr int K1_ABYTES = 32768;
constexpr int K1_STAGE = K1_ABYTES + 5 * 4096;
constexpr int K1_EXTRA = 0;
constexpr int K1_LDS = 3 * K1_STAGE;

template <int TYPE>
DI void k1_gemm(char* lds, int panel, int st0, const u16* __restrict__ x, const u16* __restrict__ w0, const u16* __restrict__ w1,
                char* __restrict__ ws) {
  constexpr int NSUB = (TYPE == 2) ? 5 : 4;
  constexpr int NI = (TYPE == 2) ? 7 : 6;
  constexpr int WROW = (TYPE == 2) ? F_ : 3 * D_;
  const int tid = threadIdx.x, lane = tid & 63, r = lane & 31, h = lane >> 5;
  const int wave = __builtin_amdgcn_readfirstlane(tid >> 6);
  const int row0 = panel * 256;

  const u16* src[NI]; int dst[NI]; int inc[NI];
#pragma unroll
  for (int i = 0; i < NI; ++i) {
    int idx = wave + 8 * i;
    if (idx >= 32 + 4 * NSUB) idx -= 4;
    if (idx < 32) {
      const int row = idx * 8 + (lane >> 3), ch = (lane & 7) ^ ((row >> 1) & 7);
      src[i] = x + (size_t)(row0 + row) * D_ + ch * 8; dst[i] = idx * 1024; inc[i] = 64;
    } else {
      const int b = idx - 32, sl = b >> 2, kr = (b & 3) * 16 + (lane >> 2), p = lane & 3;
      if (TYPE == 2) {
        int st = st0 + sl; if (st > 85) st = 85;
        src[i] = ((p & 2) ? w1 : w0) + (size_t)kr * WROW + st * 16 + (p & 1) * 8;
      } else {
        src[i] = w0 + (size_t)kr * WROW + (st0 + sl) * 32 + p * 8;
      }
      dst[i] = K1_ABYTES + b * 1024; inc[i] = 64 * WROW;
    }
  }
  auto stage = [&](int kc, int buf) {
#pragma unroll
    for (int i = 0; i < NI; ++i) glds16(src[i] + (size_t)kc * inc[i], lds + buf * K1_STAGE + dst[i]);
  };

  f32x16 acc[NSUB];
  float cs[NSUB];
#pragma unroll
  for (int s = 0; s < NSUB; ++s) { acc[s] = f32x16{}; cs[s] = 0.f; }
  float sum = 0.f, ssq = 0.f;
  const unsigned ONES = 0x3f803f80u;

  const int arow = wave * 32 + r;
  const lds_cptr abase = (lds_cptr)lds + arow * 128;
  const int aswz = (arow >> 1) & 7;
  const lds_cptr bbase = (lds_cptr)lds + K1_ABYTES + (8 * h + ((lane & 15) >> 2)) * 64 + ((lane >> 4) & 1) * 32 + (lane & 3) * 8;

  stage(0, 0); stage(1, 1);
#pragma unroll 1
  for (int kc2 = 0; kc2 < 8; kc2 += 2) {
#pragma unroll
    for (int kk = 0; kk < 2; ++kk) {
      const int kc = kc2 + kk;
      if (kc < 7) { if (NI == 7) WAITV(7); else WAITV(6); } else WAITV(0);
      BAR();
      if (kc + 2 < 8) stage(kc + 2, (kc + 2) % 3);
      const int boff = (kc % 3) * K1_STAGE;
#pragma unroll
      for (int ks = 0; ks < 4; ++ks) {
        const bf16x8 af = lds_read16(abase + boff + (((2 * ks + h) ^ aswz) << 4));
        const u32x4 au = __builtin_bit_cast(u32x4, af);
#pragma unroll
        for (int j = 0; j < 4; ++j) { sum = dot2(au[j], ONES, sum); ssq = dot2(au[j], au[j], ssq); }
        const bool mycs = (((kk * 4 + ks) & 7) == wave);
#pragma unroll
        for (int s = 0; s < NSUB; ++s) {
          const s16x4 lo = tr_read(bbase + boff + s * 4096 + ks * 1024);
          const s16x4 hi = tr_read(bbase + boff + s * 4096 + ks * 1024 + 256);
          const bf16x8 bf = __builtin_shufflevector(lo, hi, 0, 1, 2, 3, 4, 5, 6, 7);
          if (TYPE == 1) acc[s] = MFMA(af, bf, acc[s]); else acc[s] = MFMA(bf, af, acc[s]);
          if (mycs) {
            const u32x4 bu = __builtin_bit_cast(u32x4, bf);
#pragma unroll
            for (int j = 0; j < 4; ++j) cs[s] = dot2(bu[j], ONES, cs[s]);
          }
        }
      }
    }
  }
  sum = swap_add(sum); ssq = swap_add(ssq);
  const float mean = sum * (1.f / D_);
  const float var = __builtin_fmaxf(ssq * (1.f / D_) - mean * mean, 0.f);
  const float rstd = __builtin_amdgcn_rsqf(var + 1e-5f);
  float* csp = (float*)(lds + K1_EXTRA);
  float* cst = csp + 8 * 5 * 32;
  float* stt = cst + 5 * 32;
#pragma unroll
  for (int s = 0; s < NSUB; ++s) { const float t = swap_add(cs[s]); if (h == 0) csp[(wave * 5 + s) * 32 + r] = t; }
  if (TYPE == 1 && h == 0) { stt[wave * 64 + r] = mean; stt[wave * 64 + 32 + r] = rstd; }
  __syncthreads();
  if (tid < NSUB * 32) {
    float t = 0.f;
#pragma unroll
    for (int w = 0; w < 8; ++w) t += csp[w * 160 + tid];
    cst[tid] = t;
  }
  __syncthreads();
  const int mt = panel * 8 + wave;
  if (TYPE == 0) {
#pragma unroll
    for (int s = 0; s < NSUB; ++s) {
      const int st = st0 + s;
      const float sc = (st < 16) ? rstd * (0.25f * 1.4426950408889634f) : rstd;
      char* dstp = ws + ((st < 16) ? WS_Q : WS_K) + ((size_t)((st & 15) * 2) * 64 + mt) * 1024 + lane * 16;
#pragma unroll
      for (int hd = 0; hd < 2; ++hd) {
        const f32x4 c0 = *(const f32x4*)(cst + s * 32 + 16 * hd + 4 * h), c1 = *(const f32x4*)(cst + s * 32 + 16 * hd + 8 + 4 * h);
        float v[8];
#pragma unroll
        for (int j = 0; j < 4; ++j) { v[j] = (acc[s][8 * hd + j] - mean * c0[j]) * sc; v[4 + j] = (acc[s][8 * hd + 4 + j] - mean * c1[j]) * sc; }
        u32x4 o = {cvtpk(v[0], v[1]), cvtpk(v[2], v[3]), cvtpk(v[4], v[5]), cvtpk(v[6], v[7])};
        *(u32x4*)(dstp + (size_t)hd * 64 * 1024) = o;
      }
    }
  } else if (TYPE == 1) {
    f32x4 mn[4], rs[4];
#pragma unroll
    for (int g = 0; g < 4; ++g) { mn[g] = *(const f32x4*)(stt + wave * 64 + 8 * g + 4 * h); rs[g] = *(const f32x4*)(stt + wave * 64 + 32 + 8 * g + 4 * h); }
#pragma unroll
    for (int s = 0; s < NSUB; ++s) {
      const int st = st0 + s - 32;
      const float c = cst[s * 32 + r];
      char* dstp = ws + WS_V + ((size_t)(st * 2 + (r >> 4)) * 64 + mt) * 1024 + h * 256 + (r & 15) * 16;
#pragma unroll
      for (int ks = 0; ks < 2; ++ks) {
        float v[8];
#pragma unroll
        for (int j = 0; j < 4; ++j) {
          v[j] = (acc[s][8 * ks + j] - mn[2 * ks][j] * c) * rs[2 * ks][j];
          v[4 + j] = (acc[s][8 * ks + 4 + j] - mn[2 * ks + 1][j] * c) * rs[2 * ks + 1][j];
        }
        u32x4 o = {cvtpk(v[0], v[1]), cvtpk(v[2], v[3]), cvtpk(v[4], v[5]), cvtpk(v[6], v[7])};
        *(u32x4*)(dstp + ks * 512) = o;
      }
    }
  } else {
#pragma unroll
    for (int s = 0; s < NSUB; ++s) {
      const int st = st0 + s;
      if (st > 85) break;
      const f32x4 g0 = *(const f32x4*)(cst + s * 32 + 4 * h), g1 = *(const f32x4*)(cst + s * 32 + 8 + 4 * h);
      const f32x4 u0 = *(const f32x4*)(cst + s * 32 + 16 + 4 * h), u1 = *(const f32x4*)(cst + s * 32 + 24 + 4 * h);
      float v[8];
#pragma unroll
      for (int j = 0; j < 8; ++j) {
        const float g = (acc[s][j] - mean * (j < 4 ? g0[j & 3] : g1[j & 3])) * rstd;
        const float u = (acc[s][8 + j] - mean * (j < 4 ? u0[j & 3] : u1[j & 3])) * rstd;
        const float sg = __builtin_amdgcn_rcpf(1.f + __builtin_amdgcn_exp2f(-1.4426950408889634f * g));
        v[j] = g * sg * u;
      }
      u32x4 o = {cvtpk(v[0], v[1]), cvtpk(v[2], v[3]), cvtpk(v[4], v[5]), cvtpk(v[6], v[7])};
      *(u32x4*)(ws + WS_A + ((size_t)mt * KST3 + 32 + st) * 1024 + lane * 16) = o;
    }
  }
}

__global__ __launch_bounds__(512, 2) void k1_kernel(const u16* __restrict__ x, const u16* __restrict__ wqkv, const u16* __restrict__ wo,
                                                    const u16* __restrict__ wg, const u16* __restrict__ wu, const u16* __restrict__ wd,
                                                    char* __restrict__ ws) {
  __shared__ __attribute__((aligned(16))) char lds[K1_LDS];
  const int bid = blockIdx.x;
  const int xcd = bid & 7, idx = bid >> 3;
  if (idx < 30) {
    const int panel = (xcd & 1) * 4 + (idx & 3);
    const int g = idx >> 2;
    int grp, pnl;
    if (idx < 28) { grp = (xcd >> 1) + 4 * g; pnl = panel; }
    else { grp = 28 + (idx - 28); pnl = xcd; }
    if (grp < 8) k1_gemm<0>(lds, pnl, grp * 4, x, wqkv, nullptr, ws);
    else if (grp < 12) k1_gemm<1>(lds, pnl, 32 + (grp - 8) * 4, x, wqkv, nullptr, ws);
    else k1_gemm<2>(lds, pnl, (grp - 12) * 5, x, wg, wu, ws);
  } else {
    const int lane = threadIdx.x & 63, c = lane & 31, h = lane >> 5;
    const int wv = ((idx - 30) * 8 + xcd) * 8 + (threadIdx.x >> 6);
    for (int f = wv; f < KST3 * 16; f += 128) {
      const int ks = f >> 4, nt = f & 15;
      const u16* src = (ks < 32) ? wo + (size_t)ks * 16 * D_ : wd + (size_t)(ks - 32) * 16 * D_;
      src += nt * 32 + c + (size_t)(4 * h) * D_;
      unsigned e[8];
#pragma unroll
      for (int j = 0; j < 8; ++j) e[j] = src[(size_t)(8 * (j >> 2) + (j & 3)) * D_];
      u32x4 o = {e[0] | (e[1] << 16), e[2] | (e[3] << 16), e[4] | (e[5] << 16), e[6] | (e[7] << 16)};
      *(u32x4*)(ws + WS_W + (size_t)f * 1024 + lane * 16) = o;
    }
  }
}

constexpr int K2_V = 65536, K2_C = 131072, K2_LDS = K2_C + 4096 + 64;
constexpr float THR = 48.f;

__global__ __launch_bounds__(512, 2) void k2_kernel(char* __restrict__ ws) {
  __shared__ __attribute__((aligned(16))) char lds[K2_LDS];
  const int tid = threadIdx.x, lane = tid & 63, r = lane & 31, h = lane >> 5;
  const int wave = __builtin_amdgcn_readfirstlane(tid >> 6);
  const int bid = blockIdx.x;
  const int head = (bid & 7) * 4 + ((bid >> 3) & 3), qb = bid >> 5;
  const char* kg = ws + WS_K + (size_t)head * 65536 + lane * 16;
  const char* vg = ws + WS_V + (size_t)head * 65536 + lane * 16;
#pragma unroll
  for (int c = 0; c < 4; ++c) {
#pragma unroll
    for (int i = 0; i < 2; ++i) {
      const int t = c * 16 + wave * 2 + i;
      glds16(kg + t * 1024, lds + t * 1024);
      glds16(vg + t * 1024, lds + K2_V + t * 1024);
    }
  }
  if (tid < 256) { u32x4 z = {0, 0, 0, 0}; *(u32x4*)(lds + K2_C + tid * 16) = z; }
  __syncthreads();
  if (tid < 8) { u32x4 o = {0x3f803f80u, 0x3f803f80u, 0x3f803f80u, 0x3f803f80u}; *(u32x4*)(lds + K2_C + tid * 512) = o; }
  const int mt = qb * 8 + wave;
  const bf16x8 qf = *(const bf16x8*)(ws + WS_Q + ((size_t)head * 64 + mt) * 1024 + lane * 16);
  __syncthreads();

  lds_cptr kp = (lds_cptr)lds + lane * 16;
  lds_cptr vp = (r < 16) ? (lds_cptr)lds + K2_V + h * 256 + r * 16 : (lds_cptr)lds + K2_C + (r == 16 ? 0 : 16);
  const int vinc = (r < 16) ? 1024 : 0;

  f32x16 o = f32x16{}, negm = f32x16{};
  f32x16 sc = MFMA(lds_read16(kp), qf, negm);
  {
    float m = sc[0];
#pragma unroll
    for (int i = 1; i < 16; ++i) m = __builtin_fmaxf(m, sc[i]);
    m = swap_max(m);
#pragma unroll
    for (int i = 0; i < 16; ++i) { sc[i] -= m; negm[i] = -m; }
  }
#pragma unroll 1
  for (int t = 0; t < 64; ++t) {
    const bf16x8 v0 = lds_read16(vp), v1 = lds_read16(vp + 512);
    vp += vinc;
    f32x16 sn;
    if (t < 63) sn = MFMA(lds_read16(kp + (t + 1) * 1024), qf, negm);
    float p[16];
#pragma unroll
    for (int i = 0; i < 16; ++i) p[i] = __builtin_amdgcn_exp2f(sc[i]);
    u32x4 p0 = {cvtpk(p[0], p[1]), cvtpk(p[2], p[3]), cvtpk(p[4], p[5]), cvtpk(p[6], p[7])};
    u32x4 p1 = {cvtpk(p[8], p[9]), cvtpk(p[10], p[11]), cvtpk(p[12], p[13]), cvtpk(p[14], p[15])};
    o = MFMA(v0, __builtin_bit_cast(bf16x8, p0), o);
    o = MFMA(v1, __builtin_bit_cast(bf16x8, p1), o);
    if (t < 63) {
      float m = __builtin_fmaxf(__builtin_fmaxf(sn[0], sn[1]), sn[2]);
#pragma unroll
      for (int i = 3; i < 15; i += 2) m = __builtin_fmaxf(__builtin_fmaxf(m, sn[i]), sn[i + 1]);
      m = __builtin_fmaxf(m, sn[15]);
      if (__builtin_expect(__any(m > THR), 0)) {
        const float dl = __builtin_fmaxf(swap_max(m), 0.f);
        const float f = __builtin_amdgcn_exp2f(-dl);
#pragma unroll
        for (int i = 0; i < 16; ++i) { sn[i] -= dl; negm[i] -= dl; o[i] *= f; }
      }
      sc = sn;
    }
  }
  float l = (h == 0) ? o[8] : 0.f;
  l = swap_add(l);
  const float il = 1.f / l;
  u32x4 ov = {cvtpk(o[0] * il, o[1] * il), cvtpk(o[2] * il, o[3] * il), cvtpk(o[4] * il, o[5] * il), cvtpk(o[6] * il, o[7] * il)};
  *(u32x4*)(ws + WS_A + ((size_t)mt * KST3 + head) * 1024 + lane * 16) = ov;
}

constexpr int K3_ROWB = 64 * 4 + 16;
__global__ __launch_bounds__(256, 1) void k3_kernel(const u16* __restrict__ x, const char* __restrict__ ws, u16* __restrict__ out) {
  __shared__ __attribute__((aligned(16))) char lds[4 * 64 * K3_ROWB];
  const int tid = threadIdx.x, lane = tid & 63, r = lane & 31, h = lane >> 5;
  const int wave = __builtin_amdgcn_readfirstlane(tid >> 6);
  const int bid = blockIdx.x;
  const int mb = (bid & 7) * 4 + ((bid >> 3) & 3), nb = bid >> 5;
  const int k0 = wave * 30 - (wave > 2 ? 1 : 0) , nk = (wave < 2) ? 30 : 29;
  const char* ap = ws + WS_A + ((size_t)(mb * 2) * KST3 + k0) * 1024 + lane * 16;
  const char* wp = ws + WS_W + ((size_t)k0 * 16 + nb * 2) * 1024 + lane * 16;
  f32x16 acc[2][2];
#pragma unroll
  for (int i = 0; i < 2; ++i)
#pragma unroll
    for (int j = 0; j < 2; ++j) acc[i][j] = f32x16{};
  constexpr int PF = 6;
  bf16x8 a0[PF], a1[PF], b0[PF], b1[PF];
#pragma unroll
  for (int i = 0; i < PF; ++i) {
    a0[i] = *(const bf16x8*)(ap + i * 1024); a1[i] = *(const bf16x8*)(ap + (size_t)KST3 * 1024 + i * 1024);
    b0[i] = *(const bf16x8*)(wp + i * 16384); b1[i] = *(const bf16x8*)(wp + i * 16384 + 1024);
  }
#pragma unroll 1
  for (int kb = 0; kb < 30; kb += PF) {
#pragma unroll
    for (int i = 0; i < PF; ++i) {
      const int k = kb + i;
      if (k < nk) {
        const bf16x8 A0 = a0[i], A1 = a1[i], B0 = b0[i], B1 = b1[i];
        int kn = k + PF; if (kn > nk - 1) kn = nk - 1;
        a0[i] = *(const bf16x8*)(ap + kn * 1024); a1[i] = *(const bf16x8*)(ap + (size_t)KST3 * 1024 + kn * 1024);
        b0[i] = *(const bf16x8*)(wp + kn * 16384); b1[i] = *(const bf16x8*)(wp + kn * 16384 + 1024);
        acc[0][0] = MFMA(B0, A0, acc[0][0]); acc[0][1] = MFMA(B1, A0, acc[0][1]);
        acc[1][0] = MFMA(B0, A1, acc[1][0]); acc[1][1] = MFMA(B1, A1, acc[1][1]);
      }
    }
  }
  char* pw = lds + wave * 64 * K3_ROWB;
#pragma unroll
  for (int i = 0; i < 2; ++i)
#pragma unroll
    for (int j = 0; j < 2; ++j)
#pragma unroll
      for (int g = 0; g < 4; ++g) {
        f32x4 v = {acc[i][j][4 * g], acc[i][j][4 * g + 1], acc[i][j][4 * g + 2], acc[i][j][4 * g + 3]};
        *(f32x4*)(pw + (i * 32 + r) * K3_ROWB + (j * 32 + 8 * g + 4 * h) * 4) = v;
      }
  __syncthreads();
  const int m = tid >> 2, n0 = (tid & 3) * 16;
  const size_t go = (size_t)(mb * 64 + m) * D_ + nb * 64 + n0;
  const u32x4 x0 = *(const u32x4*)(x + go), x1 = *(const u32x4*)(x + go + 8);
  float s[16];
#pragma unroll
  for (int j = 0; j < 4; ++j) {
    s[2 * j] = __uint_as_float(x0[j] << 16); s[2 * j + 1] = __uint_as_float(x0[j] & 0xffff0000u);
    s[8 + 2 * j] = __uint_as_float(x1[j] << 16); s[8 + 2 * j + 1] = __uint_as_float(x1[j] & 0xffff0000u);
  }
#pragma unroll
  for (int w = 0; w < 4; ++w)
#pragma unroll
    for (int q = 0; q < 4; ++q) {
      const f32x4 v = *(const f32x4*)(lds + w * 64 * K3_ROWB + m * K3_ROWB + (n0 + 4 * q) * 4);
#pragma unroll
      for (int j = 0; j < 4; ++j) s[4 * q + j] += v[j];
    }
  u32x4 o0 = {cvtpk(s[0], s[1]), cvtpk(s[2], s[3]), cvtpk(s[4], s[5]), cvtpk(s[6], s[7])};
  u32x4 o1 = {cvtpk(s[8], s[9]), cvtpk(s[10], s[11]), cvtpk(s[12], s[13]), cvtpk(s[14], s[15])};
  *(u32x4*)(out + go) = o0; *(u32x4*)(out + go + 8) = o1;
}

extern "C" void kernel_launch(void* const* d_in, const int* in_sizes, int n_in, void* d_out, int out_size, void* d_ws, size_t ws_size,
                              hipStream_t stream) {
  const u16* x = (const u16*)d_in[0];
  const u16* wqkv = (const u16*)d_in[1];
  const u16* wo = (const u16*)d_in[2];
  const u16* wg = (const u16*)d_in[4];
  const u16* wu = (const u16*)d_in[5];
  const u16* wd = (const u16*)d_in[6];
  char* ws = (char*)d_ws;
  k1_kernel<<<256, 512, 0, stream>>>(x, wqkv, wo, wg, wu, wd, ws);
  k2_kernel<<<256, 512, 0, stream>>>(ws);
  k3_kernel<<<256, 256, 0, stream>>>(x, ws, (u16*)d_out);
}
```

```cpp
#include <hip/hip_runtime.h>
#include <hip/hip_bf16.h>

typedef short bf16x8 __attribute__((ext_vector_type(8)));
typedef short s16x4 __attribute__((ext_vector_type(4)));
typedef float f32x16 __attribute__((ext_vector_type(16)));
typedef float f32x4 __attribute__((ext_vector_type(4)));
typedef unsigned u32x4 __attribute__((ext_vector_type(4)));
typedef unsigned u32x2 __attribute__((ext_vector_type(2)));
typedef unsigned short u16;
typedef __attribute__((address_space(3))) const char* lds_cptr;
typedef __bf16 bf16x2_t __attribute__((ext_vector_type(2)));
typedef float f32x2_t __attribute__((ext_vector_type(2)));

#define DI __device__ __forceinline__
#define MFMA(a, b, c) __builtin_amdgcn_mfma_f32_32x32x16_bf16((a), (b), (c), 0, 0, 0)
#define BC(x) __builtin_bit_cast(bf16x8, x)
#define WAITV(n) asm volatile("s_waitcnt vmcnt(" #n ")" ::: "memory")
#define BAR() __builtin_amdgcn_s_barrier()
#define SB() __builtin_amdgcn_sched_barrier(0)
#define PIN(x) asm volatile("" : "+v"(x))

constexpr int S_ = 2048, D_ = 512, F_ = 1376, NH = 32;
constexpr int KST3 = 118;
constexpr size_t WS_Q = 0;
constexpr size_t WS_K = WS_Q + (size_t)NH * 64 * 1024;
constexpr size_t WS_V = WS_K + (size_t)NH * 64 * 1024;
constexpr size_t WS_A = WS_V + (size_t)NH * 64 * 1024;
constexpr size_t WS_W = WS_A + (size_t)64 * KST3 * 1024;
constexpr size_t WS_X = WS_W + (size_t)KST3 * 16 * 1024;
constexpr size_t WS_G = WS_X + (size_t)64 * 32 * 1024;

DI unsigned pk2(float a, float b) { f32x2_t v = {a, b}; return __builtin_bit_cast(unsigned, __builtin_convertvector(v, bf16x2_t)); }
DI float swap_add(float v) {
  auto rr = __builtin_amdgcn_permlane32_swap(__float_as_uint(v), __float_as_uint(v), false, false);
  return __uint_as_float(rr[0]) + __uint_as_float(rr[1]);
}
DI float swap_max(float v) {
  auto rr = __builtin_amdgcn_permlane32_swap(__float_as_uint(v), __float_as_uint(v), false, false);
  return __builtin_fmaxf(__uint_as_float(rr[0]), __uint_as_float(rr[1]));
}
DI s16x4 tr_read(lds_cptr p) {
  typedef short v4i16_t __attribute__((ext_vector_type(4)));
  return __builtin_bit_cast(s16x4, __builtin_amdgcn_ds_read_tr16_b64_v4i16((__attribute__((address_space(3))) v4i16_t*)p));
}
DI bf16x8 lds_read16(lds_cptr p) { return *(const __attribute__((address_space(3))) bf16x8*)p; }
DI void glds16(const void* g, char* lds) {
  __builtin_amdgcn_global_load_lds((const __attribute__((address_space(1))) void*)g, (__attribute__((address_space(3))) void*)lds, 16, 0, 0);
}
DI void add_s(float& a, float b) { a = a + b; asm("" : "+v"(a)); }
DI void fmac_s(float& a, float b, float c) { a = __builtin_fmaf(b, c, a); asm("" : "+v"(a)); }
DI float mul_s(float a, float b) { float r = a * b; asm("" : "+v"(r)); return r; }
DI float lo16(unsigned u) { return __uint_as_float(u << 16); }
DI float hi16(unsigned u) { return __uint_as_float(u & 0xffff0000u); }

#define ST1(i)

constexpr int K1_AB = 8192;
constexpr int K1_SLOT = 16384;
constexpr int K1_NSLOT = 9;
constexpr int K1_LDS = K1_NSLOT * K1_SLOT;
constexpr int K1_XS = 0;
constexpr int K1_CSP = 256;
constexpr int K1_CST = K1_CSP + 16 * 128;
constexpr int K1_XP = K1_CST + 128;

DI void waitv(int N) {
  if (N >= 28) WAITV(28); else if (N >= 24) WAITV(24); else if (N >= 20) WAITV(20); else if (N >= 16) WAITV(16);
  else if (N >= 12) WAITV(12); else if (N >= 8) WAITV(8); else if (N >= 4) WAITV(4); else WAITV(0);
}

DI void k1_helper(char* lds, int panel, int col0, const u16* __restrict__ x, const u16* __restrict__ w) {
  const int lane = threadIdx.x & 63;
  const int hw = __builtin_amdgcn_readfirstlane(threadIdx.x >> 6) - 8;
  const u16* src[4];
#pragma unroll
  for (int i = 0; i < 2; ++i) {
    const int row = (hw + 4 * i) * 16 + (lane >> 2), ch = (lane & 3) ^ ((row >> 2) & 3);
    src[i] = x + (size_t)(panel * 128 + row) * D_ + ch * 8;
    const int kr = (hw + 4 * i) * 4 + (lane >> 4), pc = (lane & 15) ^ ((kr & 3) << 2);
    src[2 + i] = w + (size_t)kr * (3 * D_) + col0 + pc * 8;
  }
  auto stage = [&](int kc) {
    char* d = lds + (kc % K1_NSLOT) * K1_SLOT + hw * 1024;
    glds16(src[0] + kc * 32, d); glds16(src[1] + kc * 32, d + 4096);
    glds16(src[2] + (size_t)kc * 32 * (3 * D_), d + K1_AB); glds16(src[3] + (size_t)kc * 32 * (3 * D_), d + K1_AB + 4096);
  };
  const int g = lane >> 4, pc = lane & 15;
  const lds_cptr cr = (lds_cptr)lds + K1_AB + (8 * hw + g) * 256 + ((pc ^ (g << 2)) << 4);
  float cs[8];
#pragma unroll
  for (int c = 0; c < 8; ++c) cs[c] = 0.f;
#pragma unroll
  for (int c = 0; c < 8; ++c) stage(c);
  waitv(28);
  BAR();
#pragma unroll
  for (int kc = 0; kc < 16; ++kc) {
    waitv((kc <= 8) ? 24 : (kc >= 14 ? 0 : (14 - kc) * 4));
    BAR();
    if (kc < 8) stage(kc + 8);
    const u32x4 b0 = __builtin_bit_cast(u32x4, lds_read16(cr + (kc % K1_NSLOT) * K1_SLOT));
    const u32x4 b1 = __builtin_bit_cast(u32x4, lds_read16(cr + (kc % K1_NSLOT) * K1_SLOT + 1024));
#pragma unroll
    for (int j = 0; j < 4; ++j) {
      add_s(cs[2 * j], lo16(b0[j])); add_s(cs[2 * j + 1], hi16(b0[j]));
      add_s(cs[2 * j], lo16(b1[j])); add_s(cs[2 * j + 1], hi16(b1[j]));
    }
  }
  float* sc = (float*)lds;
  {
    f32x4 c0 = {cs[0], cs[1], cs[2], cs[3]}, c1 = {cs[4], cs[5], cs[6], cs[7]};
    float* d = sc + K1_CSP + (4 * hw + g) * 128 + pc * 8;
    *(f32x4*)d = c0; *(f32x4*)(d + 4) = c1;
  }
  __syncthreads();
  if (hw < 2) {
    const int i = hw * 64 + lane;
    float t = 0.f;
#pragma unroll
    for (int q = 0; q < 16; ++q) t += sc[K1_CSP + q * 128 + i];
    sc[K1_CST + i] = t;
  } else {
    const int i = (hw - 2) * 64 + lane;
    const float* p = sc + K1_XP + i;
    const float mean = ((p[0] + p[256]) + (p[512] + p[768])) * (1.f / D_);
    const float var = __builtin_fmaxf(((p[128] + p[384]) + (p[640] + p[896])) * (1.f / D_) - mean * mean, 0.f);
    sc[K1_XS + i] = mean; sc[K1_XS + 128 + i] = __builtin_amdgcn_rsqf(var + 1e-5f);
  }
  __syncthreads();
}

template <int TYPE, int CW>
DI void k1_gemm(char* lds, int panel, int st0, char* __restrict__ ws) {
  const int tid = threadIdx.x, lane = tid & 63, r = lane & 31, h = lane >> 5;
  const int mw = __builtin_amdgcn_readfirstlane(tid >> 6) & 1;
  f32x16 acc[2] = {f32x16{}, f32x16{}};
  const int arow = mw * 64 + r;
  const int aswz = (arow >> 2) & 3;
  const lds_cptr ab0 = (lds_cptr)lds + arow * 64 + ((h ^ aswz) << 4), ab1 = (lds_cptr)lds + arow * 64 + (((2 + h) ^ aswz) << 4);
  const lds_cptr bb = (lds_cptr)lds + K1_AB + (8 * h + ((lane & 15) >> 2)) * 256 + (CW ^ ((lane & 15) >> 2)) * 64 + ((lane >> 4) & 1) * 32 + (lane & 3) * 8;
  bf16x8 af[2][2]; s16x4 bl[2], bh[2];
  float sm[2][2] = {{0.f, 0.f}, {0.f, 0.f}}, sq[2][2] = {{0.f, 0.f}, {0.f, 0.f}};
#define K1_FRAGS(buf, soff, ks) do { __builtin_amdgcn_s_waitcnt(0xC07F); const lds_cptr a_ = ((ks) ? ab1 : ab0) + (soff); \
    af[buf][0] = lds_read16(a_); af[buf][1] = lds_read16(a_ + 2048); \
    bl[buf] = tr_read(bb + (soff) + (ks) * 4096); bh[buf] = tr_read(bb + (soff) + (ks) * 4096 + 1024); SB(); } while (0)
#define K1_MMA(buf) do { const bf16x8 bf = __builtin_shufflevector(bl[buf], bh[buf], 0, 1, 2, 3, 4, 5, 6, 7); \
    _Pragma("unroll") for (int mi = 0; mi < 2; ++mi) { \
      if (TYPE == 1) acc[mi] = MFMA(af[buf][mi], bf, acc[mi]); else acc[mi] = MFMA(bf, af[buf][mi], acc[mi]); \
      const unsigned u = __builtin_bit_cast(u32x4, af[buf][mi])[CW]; const float lo = lo16(u), hi = hi16(u); \
      add_s(sm[mi][0], lo); add_s(sm[mi][1], hi); fmac_s(sq[mi][0], lo, lo); fmac_s(sq[mi][1], hi, hi); \
      asm volatile("" : "+v"(sm[mi][0]), "+v"(sm[mi][1]), "+v"(sq[mi][0]), "+v"(sq[mi][1])); SB(); } } while (0)
  ST1(0);
  BAR();
  ST1(1);
  K1_FRAGS(0, 0, 0);
  int scur = 0, snext = K1_SLOT;
#pragma unroll 1
  for (int kc = 0; kc < 16; ++kc) {
    BAR();
    K1_FRAGS(1, scur, 1);
    K1_MMA(0);
    K1_FRAGS(0, snext, 0);
    K1_MMA(1);
    scur = snext; snext = (snext == (K1_NSLOT - 1) * K1_SLOT) ? 0 : snext + K1_SLOT;
  }
  ST1(17);
#pragma unroll
  for (int mi = 0; mi < 2; ++mi) {
    const float t = swap_add(sm[mi][0] + sm[mi][1]), u = swap_add(sq[mi][0] + sq[mi][1]);
    float* xp = (float*)lds + K1_XP + CW * 256 + mw * 64 + mi * 32 + r;
    if (h == 0) { xp[0] = t; xp[128] = u; }
  }
  __syncthreads();
  __syncthreads();
  const float* xs = (const float*)lds + K1_XS;
  const float* cp = (const float*)lds + K1_CST + CW * 32;
  const int st = st0 + CW;
#pragma unroll
  for (int mi = 0; mi < 2; ++mi) {
    const int mt = panel * 4 + mw * 2 + mi;
    const int lr = mw * 64 + mi * 32;
    if (TYPE == 0) {
      const float mean = xs[lr + r], rstd = xs[128 + lr + r];
      const float sc = (st < 16) ? rstd * (0.25f * 1.4426950408889634f) : rstd;
      char* dstp = ws + ((st < 16) ? WS_Q : WS_K) + ((size_t)((st & 15) * 2) * 64 + mt) * 1024 + lane * 16;
#pragma unroll
      for (int hd = 0; hd < 2; ++hd) {
        const f32x4 c0 = *(const f32x4*)(cp + 16 * hd + 4 * h), c1 = *(const f32x4*)(cp + 16 * hd + 8 + 4 * h);
        float v[8];
#pragma unroll
        for (int j = 0; j < 4; ++j) { v[j] = (acc[mi][8 * hd + j] - mean * c0[j]) * sc; v[4 + j] = (acc[mi][8 * hd + 4 + j] - mean * c1[j]) * sc; }
        u32x4 o = {pk2(v[0], v[1]), pk2(v[2], v[3]), pk2(v[4], v[5]), pk2(v[6], v[7])};
        *(u32x4*)(dstp + (size_t)hd * 64 * 1024) = o;
      }
    } else {
      const float c = cp[r];
      char* dstp = ws + WS_V + ((size_t)((st - 32) * 2 + (r >> 4)) * 64 + mt) * 1024 + h * 256 + (r & 15) * 16;
#pragma unroll
      for (int ks = 0; ks < 2; ++ks) {
        const f32x4 m0 = *(const f32x4*)(xs + lr + 16 * ks + 4 * h), m1 = *(const f32x4*)(xs + lr + 16 * ks + 8 + 4 * h);
        const f32x4 r0 = *(const f32x4*)(xs + 128 + lr + 16 * ks + 4 * h), r1 = *(const f32x4*)(xs + 128 + lr + 16 * ks + 8 + 4 * h);
        float v[8];
#pragma unroll
        for (int j = 0; j < 4; ++j) { v[j] = (acc[mi][8 * ks + j] - m0[j] * c) * r0[j]; v[4 + j] = (acc[mi][8 * ks + 4 + j] - m1[j] * c) * r1[j]; }
        u32x4 o = {pk2(v[0], v[1]), pk2(v[2], v[3]), pk2(v[4], v[5]), pk2(v[6], v[7])};
        *(u32x4*)(dstp + ks * 512) = o;
      }
    }
  }
  ST1(18);
}

__global__ __launch_bounds__(768, 3) void k1_kernel(const u16* __restrict__ x, const u16* __restrict__ wqkv, const u16* __restrict__ wo,
                                                    const u16* __restrict__ wg, const u16* __restrict__ wu, const u16* __restrict__ wd,
                                                    char* __restrict__ ws) {
  __shared__ __attribute__((aligned(16))) char lds[K1_LDS];
  const int bid = blockIdx.x;
  const int xcd = bid & 7, idx = bid >> 3;
  const int wave = __builtin_amdgcn_readfirstlane(threadIdx.x >> 6);
  const int lane = threadIdx.x & 63, c = lane & 31, h = lane >> 5;
  if (idx < 24) {
    const int panel = (xcd & 3) * 4 + (idx & 3), grp = (xcd >> 2) * 6 + (idx >> 2);
    if (wave >= 8) k1_helper(lds, panel, grp * 128, x, wqkv);
    else if (grp < 8) {
      switch (wave >> 1) {
        case 0: k1_gemm<0, 0>(lds, panel, grp * 4, ws); break; case 1: k1_gemm<0, 1>(lds, panel, grp * 4, ws); break;
        case 2: k1_gemm<0, 2>(lds, panel, grp * 4, ws); break; default: k1_gemm<0, 3>(lds, panel, grp * 4, ws); break;
      }
    } else {
      switch (wave >> 1) {
        case 0: k1_gemm<1, 0>(lds, panel, grp * 4, ws); break; case 1: k1_gemm<1, 1>(lds, panel, grp * 4, ws); break;
        case 2: k1_gemm<1, 2>(lds, panel, grp * 4, ws); break; default: k1_gemm<1, 3>(lds, panel, grp * 4, ws); break;
      }
    }
  } else {
    const int pb = (idx - 24) * 8 + xcd;
    if (wave < 4) {
      u32x4 v[8];
#pragma unroll
      for (int i = 0; i < 8; ++i) v[i] = *(const u32x4*)(x + (size_t)(pb * 32 + wave * 8 + i) * D_ + lane * 8);
      float f[8][8], sm[8];
#pragma unroll
      for (int i = 0; i < 8; ++i) {
#pragma unroll
        for (int j = 0; j < 4; ++j) { f[i][2 * j] = lo16(v[i][j]); f[i][2 * j + 1] = hi16(v[i][j]); }
        sm[i] = ((f[i][0] + f[i][1]) + (f[i][2] + f[i][3])) + ((f[i][4] + f[i][5]) + (f[i][6] + f[i][7]));
      }
#pragma unroll
      for (int d = 1; d < 64; d <<= 1)
#pragma unroll
        for (int i = 0; i < 8; ++i) sm[i] += __shfl_xor(sm[i], d);
#pragma unroll
      for (int i = 0; i < 8; ++i) {
        const float mean = sm[i] * (1.f / D_);
        float q = 0.f;
#pragma unroll
        for (int j = 0; j < 8; ++j) { f[i][j] -= mean; q += f[i][j] * f[i][j]; }
        sm[i] = q;
      }
#pragma unroll
      for (int d = 1; d < 64; d <<= 1)
#pragma unroll
        for (int i = 0; i < 8; ++i) sm[i] += __shfl_xor(sm[i], d);
#pragma unroll
      for (int i = 0; i < 8; ++i) {
        const float rstd = __builtin_amdgcn_rsqf(sm[i] * (1.f / D_) + 1e-5f);
        u32x4 o;
#pragma unroll
        for (int j = 0; j < 4; ++j) o[j] = pk2(f[i][2 * j] * rstd, f[i][2 * j + 1] * rstd);
        *(u32x4*)(ws + WS_X + ((size_t)pb * 32 + (lane >> 1)) * 1024 + ((lane & 1) * 32 + wave * 8 + i) * 16) = o;
      }
    } else {
      constexpr int NG = 86 * 32, NF = NG + KST3 * 16;
      const int wv = pb * 8 + wave - 4;
      char* wl = lds + wave * 10240;
      const int kr = lane >> 2, p = lane & 3;
#pragma unroll
      for (int i = 0; i < 10; ++i) {
        int f = wv + i * 512; if (f > NF - 1) f = NF - 1;
        const u16* src;
        if (f < NG) { const int st = f >> 5, ks = f & 31; src = ((p < 2) ? wg : wu) + (size_t)(ks * 16 + kr) * F_ + st * 16 + (p & 1) * 8; }
        else { const int g = f - NG, ks = g >> 4, nt = g & 15; src = ((ks < 32) ? wo + (size_t)(ks * 16 + kr) * D_ : wd + (size_t)((ks - 32) * 16 + kr) * D_) + nt * 32 + p * 8; }
        glds16(src, wl + i * 1024);
      }
      WAITV(0);
      const lds_cptr tb = (lds_cptr)wl + ((lane & 15) >> 2) * 64 + ((lane >> 4) & 1) * 32 + (lane & 3) * 8;
#pragma unroll
      for (int i = 0; i < 10; ++i) {
        const int f = wv + i * 512;
        const bool nat = f < NG;
        const s16x4 lo = tr_read(tb + i * 1024 + (nat ? 8 * h : 4 * h) * 64), hi = tr_read(tb + i * 1024 + (nat ? 8 * h + 4 : 8 + 4 * h) * 64);
        const bf16x8 o = __builtin_shufflevector(lo, hi, 0, 1, 2, 3, 4, 5, 6, 7);
        char* d = nat ? ws + WS_G + (size_t)f * 1024 : ws + WS_W + (size_t)(f - NG) * 1024;
        if (f < NF) *(bf16x8*)(d + lane * 16) = o;
      }
    }
  }
}

constexpr int K2_V = 65536, K2_C = 131072, K2_Q = K2_C + 8192, K2_CNT = K2_Q + 8192, K2_LDS = K2_CNT + 64;
constexpr float HEAD = 96.f;
#define K2_ITERS 16

DI float cexp2(float x) { return __builtin_amdgcn_fmed3f(__builtin_amdgcn_exp2f(x), 0.f, 1.f); }
DI void exp_pack(const f32x16& s, u32x4& p0, u32x4& p1) {
#pragma unroll
  for (int i = 0; i < 4; ++i) { p0[i] = pk2(cexp2(s[2 * i]), cexp2(s[2 * i + 1])); p1[i] = pk2(cexp2(s[8 + 2 * i]), cexp2(s[9 + 2 * i])); }
}
DI float max16(const f32x16& s) {
  float m = __builtin_fmaxf(__builtin_fmaxf(s[0], s[1]), s[2]);
#pragma unroll
  for (int i = 3; i < 15; i += 2) m = __builtin_fmaxf(__builtin_fmaxf(m, s[i]), s[i + 1]);
  return __builtin_fmaxf(m, s[15]);
}
DI f32x16 splat16(float v) { f32x16 r;
#pragma unroll
  for (int i = 0; i < 16; ++i) r[i] = v;
  return r; }

DI void k2_fix(f32x16& o, float& nm, const bf16x8 qf, lds_cptr kp0, lds_cptr vp0, int vinc, int t0, int h) {
  float sm = -3.0e38f;
#pragma unroll 1
  for (int t = t0; t < t0 + 4; ++t) {
    const f32x16 s = MFMA(lds_read16(kp0 + t * 1024), qf, splat16(nm));
    sm = __builtin_fmaxf(sm, max16(s));
    u32x4 p0, p1; exp_pack(s, p0, p1);
#pragma unroll
    for (int i = 0; i < 4; ++i) { p0[i] ^= 0x80008000u; p1[i] ^= 0x80008000u; }
    o = MFMA(lds_read16(vp0 + t * vinc), BC(p0), o);
    o = MFMA(lds_read16(vp0 + t * vinc + 512), BC(p1), o);
  }
  sm = swap_max(sm);
  const float lo = swap_add(h == 0 ? o[8] : 0.f);
  sm = __builtin_fmaxf(sm, __builtin_amdgcn_logf(__builtin_fmaxf(lo, 1e-37f)));
  const float dl = __builtin_fmaxf(sm + HEAD, 0.f);
  const float f = __builtin_amdgcn_exp2f(-dl);
#pragma unroll
  for (int i = 0; i < 16; ++i) o[i] *= f;
  nm -= dl;
#pragma unroll 1
  for (int t = t0; t < t0 + 4; ++t) {
    const f32x16 s = MFMA(lds_read16(kp0 + t * 1024), qf, splat16(nm));
    u32x4 p0, p1; exp_pack(s, p0, p1);
    o = MFMA(lds_read16(vp0 + t * vinc), BC(p0), o);
    o = MFMA(lds_read16(vp0 + t * vinc + 512), BC(p1), o);
  }
}
DI void k2_sync(char* lds, int target) {
  volatile __attribute__((address_space(3))) unsigned* cnt = (volatile __attribute__((address_space(3))) unsigned*)(lds + K2_CNT);
  if ((threadIdx.x & 63) == 0) __hip_atomic_fetch_add((__attribute__((address_space(3))) unsigned*)(lds + K2_CNT), 1u, __ATOMIC_RELAXED, __HIP_MEMORY_SCOPE_WORKGROUP);
  while ((int)__builtin_amdgcn_readfirstlane(*cnt) < target) __builtin_amdgcn_s_sleep(1);
  asm volatile("" ::: "memory");
}

DI void k2_attn(char* lds, char* __restrict__ ws) {
  const int tid = threadIdx.x, lane = tid & 63, r = lane & 31, h = lane >> 5;
  const int wave = __builtin_amdgcn_readfirstlane(tid >> 6);
  const int bid = blockIdx.x;
  const int head = (bid & 7) * 4 + ((bid >> 3) & 3), qb = bid >> 5;
  const int mt = qb * 8 + wave;
  {
    const char* kg = ws + WS_K + (size_t)head * 65536 + wave * 1024 + lane * 16;
    const char* vg = ws + WS_V + (size_t)head * 65536 + wave * 1024 + lane * 16;
    glds16(ws + WS_Q + ((size_t)head * 64 + mt) * 1024 + lane * 16, lds + K2_Q + wave * 1024);
#pragma unroll
    for (int c = 0; c < 8; ++c) {
      glds16(kg + c * 8192, lds + c * 8192 + wave * 1024);
      glds16(vg + c * 8192, lds + K2_V + c * 8192 + wave * 1024);
    }
  }
  {
    const unsigned c = ((tid & 31) == 0) ? 0x3f803f80u : 0u;
    u32x4 z = {c, c, c, c}; *(u32x4*)(lds + K2_C + tid * 16) = z;
  }
  asm volatile("s_waitcnt vmcnt(12) lgkmcnt(0)" ::: "memory");
  k2_sync(lds, 8);
  const bf16x8 qf = lds_read16((lds_cptr)lds + K2_Q + wave * 1024 + lane * 16);
  const lds_cptr kp0 = (lds_cptr)lds + lane * 16;
  const lds_cptr vp0 = (r < 16) ? (lds_cptr)lds + K2_V + h * 256 + r * 16 : (lds_cptr)lds + K2_C + (r == 16 ? 0 : 16);
  const int vinc = (r < 16) ? 1024 : 0;
  f32x16 o = f32x16{};
  float nm;
  {
    const f32x16 z = f32x16{};
    float m = max16(MFMA(lds_read16(kp0), qf, z));
#pragma unroll
    for (int t = 1; t < 4; ++t) m = __builtin_fmaxf(m, max16(MFMA(lds_read16(kp0 + t * 1024), qf, z)));
    nm = -(swap_max(m) + HEAD);
  }
#define EXP4(S, b) do { _Pragma("unroll") for (int i = 0; i < 4; ++i) e[i] = cexp2(S[b + i]); PIN(e); SB(); } while (0)
#define EXP4P(P, S, b) do { P[0] = pk2(e[0], e[1]); P[1] = pk2(e[2], e[3]); \
    P[2] = pk2(cexp2(S[b]), cexp2(S[b + 1])); P[3] = pk2(cexp2(S[b + 2]), cexp2(S[b + 3])); PIN(P); SB(); } while (0)
#define THEAD(j, SC, SN, KN, KL, VL0, VL1) do { \
    EXP4(SC, 0); \
    SN = MFMA(KN, qf, SN); PIN(SN); SB(); \
    KL = lds_read16(kp + (j + 2) * 1024); VL0 = lds_read16(vp + (j + 1) * 1024); VL1 = lds_read16(vp + (j + 1) * 1024 + 512); SB(); \
    EXP4P(p0, SC, 4); } while (0)
#define TTAIL(SC, SN, KO, VU0, VU1) do { \
    o = MFMA(VU0, BC(p0), o); PIN(o); SB(); \
    EXP4(SC, 8); \
    SN = MFMA(KO, nqf, SN); PIN(SN); SB(); \
    EXP4P(p1, SC, 12); \
    o = MFMA(VU1, BC(p1), o); PIN(o); SB(); } while (0)
  f32x4 e;
  bf16x8 nqf;
  { u32x4 t = __builtin_bit_cast(u32x4, qf); t[0] ^= 0x80008000u; t[1] ^= 0x80008000u; t[2] ^= 0x80008000u; t[3] ^= 0x80008000u; nqf = BC(t); }
  int it = 0;
#pragma unroll 1
  for (;;) {
    lds_cptr kp = kp0 + it * 4096, vp = vp0 + it * 4 * vinc;
    bf16x8 k0 = lds_read16(kp), k1 = lds_read16(kp + 1024), k2, k3 = k0;
    bf16x8 vA0 = lds_read16(vp), vA1 = lds_read16(vp + 512), vB0, vB1;
    f32x16 sA = MFMA(k0, qf, splat16(nm)), sB = sA;
    u32x4 p0, p1;
    THEAD(0, sA, sB, k1, k2, vB0, vB1);
    bool trig;
#pragma unroll 1
    do {
      TTAIL(sA, sB, k3, vA0, vA1);
      THEAD(1, sB, sA, k2, k3, vA0, vA1); TTAIL(sB, sA, k0, vB0, vB1);
      THEAD(2, sA, sB, k3, k0, vB0, vB1); TTAIL(sA, sB, k1, vA0, vA1);
      THEAD(3, sB, sA, k0, k1, vA0, vA1); TTAIL(sB, sA, k2, vB0, vB1);
      kp += 4096; vp += 4 * vinc; ++it;
      if (it == 2) { WAITV(8); k2_sync(lds, 16); }
      if (it == 6) { WAITV(0); k2_sync(lds, 24); }
      THEAD(0, sA, sB, k1, k2, vB0, vB1);
      trig = __any(o[8] >= 1.f);
    } while (it < K2_ITERS && !trig);
    if (__builtin_expect(!trig, 1)) break;
    k2_fix(o, nm, qf, kp0, vp0, vinc, 4 * it - 4, h);
    if (it >= K2_ITERS) break;
  }
  asm volatile("s_nop 15" : "+v"(o));
  const float il = 1.f / swap_add(h == 0 ? o[8] : 0.f);
  u32x4 ov = {pk2(mul_s(o[0], il), mul_s(o[1], il)), pk2(mul_s(o[2], il), mul_s(o[3], il)), pk2(mul_s(o[4], il), mul_s(o[5], il)), pk2(mul_s(o[6], il), mul_s(o[7], il))};
  *(u32x4*)(ws + WS_A + ((size_t)mt * KST3 + head) * 1024 + lane * 16) = ov;
}

DI void k2_ffn(char* __restrict__ ws) {
  const int lane = threadIdx.x & 63;
  const int fw = __builtin_amdgcn_readfirstlane(threadIdx.x >> 6) - 8;
  const int bid = blockIdx.x;
  const int sp = (bid & 7) + 8 * fw, mp = bid >> 3;
  if (sp < 43) {
    const __amdgpu_buffer_rsrc_t rs = __builtin_amdgcn_make_buffer_rsrc(ws, 0, 0x7fffffff, 0x00020000);
    const int xo = (int)WS_X + mp * 65536, wo = (int)WS_G + sp * 65536, l16 = lane * 16;
    f32x16 acc[2][2] = {{f32x16{}, f32x16{}}, {f32x16{}, f32x16{}}};
#define FF_PF 3
    constexpr int PF = FF_PF;
    u32x4 a0[PF], a1[PF], b0[PF], b1[PF];
#define BLD(off) __builtin_amdgcn_raw_buffer_load_b128(rs, l16, (off), 0)
#define FF_LOAD(i, k) do { a0[i] = BLD(xo + (k) * 1024); a1[i] = BLD(xo + 32768 + (k) * 1024); \
    b0[i] = BLD(wo + (k) * 1024); b1[i] = BLD(wo + 32768 + (k) * 1024); } while (0)
#pragma unroll
    for (int i = 0; i < PF; ++i) FF_LOAD(i, i);
#pragma unroll
    for (int k = 0; k < 32; ++k) {
      const int i = k % PF;
      acc[0][0] = MFMA(BC(b0[i]), BC(a0[i]), acc[0][0]); acc[0][1] = MFMA(BC(b1[i]), BC(a0[i]), acc[0][1]);
      acc[1][0] = MFMA(BC(b0[i]), BC(a1[i]), acc[1][0]); acc[1][1] = MFMA(BC(b1[i]), BC(a1[i]), acc[1][1]);
      PIN(acc[1][1]); SB();
      if (k + PF < 32) FF_LOAD(i, k + PF);
      SB();
    }
    asm volatile("s_nop 15\n\ts_nop 15" : "+v"(acc[0][0]), "+v"(acc[0][1]), "+v"(acc[1][0]), "+v"(acc[1][1]));
    SB();
#pragma unroll
    for (int mi = 0; mi < 2; ++mi)
#pragma unroll
      for (int s = 0; s < 2; ++s) {
        float v[8];
#pragma unroll
        for (int j = 0; j < 8; ++j) {
          const float g = acc[mi][s][j], u = acc[mi][s][8 + j];
          float d = __builtin_amdgcn_exp2f(mul_s(g, -1.4426950408889634f)); add_s(d, 1.f);
          v[j] = mul_s(mul_s(g, __builtin_amdgcn_rcpf(d)), u);
        }
        u32x4 o = {pk2(v[0], v[1]), pk2(v[2], v[3]), pk2(v[4], v[5]), pk2(v[6], v[7])};
        *(u32x4*)(ws + WS_A + ((size_t)(mp * 2 + mi) * KST3 + 32 + sp * 2 + s) * 1024 + lane * 16) = o;
      }
  }
}

__global__ __launch_bounds__(1024, 4) void k2_kernel(char* __restrict__ ws) {
  __shared__ __attribute__((aligned(16))) char lds[K2_LDS];
  if (threadIdx.x == 0) *(unsigned*)(lds + K2_CNT) = 0u;
  __syncthreads();
  if (__builtin_amdgcn_readfirstlane(threadIdx.x) < 512) k2_attn(lds, ws); else k2_ffn(ws);
}

constexpr int K3_ROWB = 64 * 4 + 16;
__global__ __launch_bounds__(256, 1) void k3_kernel(const u16* __restrict__ x, const char* __restrict__ ws, u16* __restrict__ out) {
  __shared__ __attribute__((aligned(16))) char lds[4 * 64 * K3_ROWB];
  const int tid = threadIdx.x, lane = tid & 63, r = lane & 31, h = lane >> 5;
  const int wave = __builtin_amdgcn_readfirstlane(tid >> 6);
  const int bid = blockIdx.x;
  const int mb = (bid & 7) * 4 + ((bid >> 3) & 3), nb = bid >> 5;
  const int k0 = wave * 30 - (wave > 2 ? 1 : 0) , nk = (wave < 2) ? 30 : 29;
  const char* ap = ws + WS_A + ((size_t)(mb * 2) * KST3 + k0) * 1024 + lane * 16;
  const char* wp = ws + WS_W + ((size_t)k0 * 16 + nb * 2) * 1024 + lane * 16;
  f32x16 acc[2][2];
#pragma unroll
  for (int i = 0; i < 2; ++i)
#pragma unroll
    for (int j = 0; j < 2; ++j) acc[i][j] = f32x16{};
  constexpr int PF = 12;
  u32x4 a0[PF], a1[PF], b0[PF], b1[PF];
  const int klast = nk - 1;
#define K3_LOAD(i, k) do { const int kk_ = ((k) < 29) ? (k) : klast; \
    a0[i] = *(const u32x4*)(ap + kk_ * 1024); a1[i] = *(const u32x4*)(ap + (size_t)KST3 * 1024 + kk_ * 1024); \
    b0[i] = *(const u32x4*)(wp + kk_ * 16384); b1[i] = *(const u32x4*)(wp + kk_ * 16384 + 1024); } while (0)
#pragma unroll
  for (int i = 0; i < PF; ++i) K3_LOAD(i, i);
#pragma unroll
  for (int k = 0; k < 30; ++k) {
    const int i = k % PF;
    if (k < 29 || nk == 30) {
      acc[0][0] = MFMA(BC(b0[i]), BC(a0[i]), acc[0][0]); acc[0][1] = MFMA(BC(b1[i]), BC(a0[i]), acc[0][1]);
      acc[1][0] = MFMA(BC(b0[i]), BC(a1[i]), acc[1][0]); acc[1][1] = MFMA(BC(b1[i]), BC(a1[i]), acc[1][1]);
    }
    if (k + PF < 30) K3_LOAD(i, k + PF);
    __builtin_amdgcn_sched_barrier(0);
  }
  char* pw = lds + wave * 64 * K3_ROWB;
#pragma unroll
  for (int i = 0; i < 2; ++i)
#pragma unroll
    for (int j = 0; j < 2; ++j)
#pragma unroll
      for (int g = 0; g < 4; ++g) {
        f32x4 v = {acc[i][j][4 * g], acc[i][j][4 * g + 1], acc[i][j][4 * g + 2], acc[i][j][4 * g + 3]};
        *(f32x4*)(pw + (i * 32 + r) * K3_ROWB + (j * 32 + 8 * g + 4 * h) * 4) = v;
      }
  __syncthreads();
  const int m = tid >> 2, n0 = (tid & 3) * 16;
  const size_t go = (size_t)(mb * 64 + m) * D_ + nb * 64 + n0;
  const u32x4 x0 = *(const u32x4*)(x + go), x1 = *(const u32x4*)(x + go + 8);
  float s[16];
#pragma unroll
  for (int j = 0; j < 4; ++j) {
    s[2 * j] = __uint_as_float(x0[j] << 16); s[2 * j + 1] = __uint_as_float(x0[j] & 0xffff0000u);
    s[8 + 2 * j] = __uint_as_float(x1[j] << 16); s[8 + 2 * j + 1] = __uint_as_float(x1[j] & 0xffff0000u);
  }
#pragma unroll
  for (int w = 0; w < 4; ++w)
#pragma unroll
    for (int q = 0; q < 4; ++q) {
      const f32x4 v = *(const f32x4*)(lds + w * 64 * K3_ROWB + m * K3_ROWB + (n0 + 4 * q) * 4);
#pragma unroll
      for (int j = 0; j < 4; ++j) s[4 * q + j] += v[j];
    }
  u32x4 o0 = {pk2(s[0], s[1]), pk2(s[2], s[3]), pk2(s[4], s[5]), pk2(s[6], s[7])};
  u32x4 o1 = {pk2(s[8], s[9]), pk2(s[10], s[11]), pk2(s[12], s[13]), pk2(s[14], s[15])};
  *(u32x4*)(out + go) = o0; *(u32x4*)(out + go + 8) = o1;
}

extern "C" void kernel_launch(void* const* d_in, const int* in_sizes, int n_in, void* d_out, int out_size, void* d_ws, size_t ws_size,
                              hipStream_t stream) {
  const u16* x = (const u16*)d_in[0];
  const u16* wqkv = (const u16*)d_in[1];
  const u16* wo = (const u16*)d_in[2];
  const u16* wg = (const u16*)d_in[4];
  const u16* wu = (const u16*)d_in[5];
  const u16* wd = (const u16*)d_in[6];
  char* ws = (char*)d_ws;
  k1_kernel<<<256, 768, 0, stream>>>(x, wqkv, wo, wg, wu, wd, ws);
  k2_kernel<<<256, 1024, 0, stream>>>(ws);
  k3_kernel<<<256, 256, 0, stream>>>(x, ws, (u16*)d_out);
}
```

```cpp
#include <hip/hip_runtime.h>
#include <hip/hip_bf16.h>

typedef short bf16x8 __attribute__((ext_vector_type(8)));
typedef short s16x4 __attribute__((ext_vector_type(4)));
typedef float f32x16 __attribute__((ext_vector_type(16)));
typedef float f32x4 __attribute__((ext_vector_type(4)));
typedef unsigned u32x4 __attribute__((ext_vector_type(4)));
typedef unsigned u32x2 __attribute__((ext_vector_type(2)));
typedef unsigned short u16;
typedef __attribute__((address_space(3))) const char* lds_cptr;
typedef __bf16 bf16x2_t __attribute__((ext_vector_type(2)));
typedef float f32x2_t __attribute__((ext_vector_type(2)));

#define DI __device__ __forceinline__
#define MFMA(a, b, c) __builtin_amdgcn_mfma_f32_32x32x16_bf16((a), (b), (c), 0, 0, 0)
#define BC(x) __builtin_bit_cast(bf16x8, x)
#define WAITV(n) asm volatile("s_waitcnt vmcnt(" #n ")" ::: "memory")
#define BAR() __builtin_amdgcn_s_barrier()
#define SB() __builtin_amdgcn_sched_barrier(0)
#define PIN(x) asm volatile("" : "+v"(x))

constexpr int S_ = 2048, D_ = 512, F_ = 1376, NH = 32;
constexpr int KST3 = 118;
constexpr size_t WS_Q = 0;
constexpr size_t WS_K = WS_Q + (size_t)NH * 64 * 1024;
constexpr size_t WS_V = WS_K + (size_t)NH * 64 * 1024;
constexpr size_t WS_A = WS_V + (size_t)NH * 64 * 1024;
constexpr size_t WS_W = WS_A + (size_t)64 * KST3 * 1024;
constexpr size_t WS_X = WS_W + (size_t)KST3 * 16 * 1024;
constexpr size_t WS_G = WS_X + (size_t)64 * 32 * 1024;

DI unsigned pk2(float a, float b) { f32x2_t v = {a, b}; return __builtin_bit_cast(unsigned, __builtin_convertvector(v, bf16x2_t)); }
DI float swap_add(float v) {
  auto rr = __builtin_amdgcn_permlane32_swap(__float_as_uint(v), __float_as_uint(v), false, false);
  return __uint_as_float(rr[0]) + __uint_as_float(rr[1]);
}
DI float swap_max(float v) {
  auto rr = __builtin_amdgcn_permlane32_swap(__float_as_uint(v), __float_as_uint(v), false, false);
  return __builtin_fmaxf(__uint_as_float(rr[0]), __uint_as_float(rr[1]));
}
DI s16x4 tr_read(lds_cptr p) {
  typedef short v4i16_t __attribute__((ext_vector_type(4)));
  return __builtin_bit_cast(s16x4, __builtin_amdgcn_ds_read_tr16_b64_v4i16((__attribute__((address_space(3))) v4i16_t*)p));
}
DI bf16x8 lds_read16(lds_cptr p) { return *(const __attribute__((address_space(3))) bf16x8*)p; }
DI void glds16(const void* g, char* lds) {
  __builtin_amdgcn_global_load_lds((const __attribute__((address_space(1))) void*)g, (__attribute__((address_space(3))) void*)lds, 16, 0, 0);
}
DI void add_s(float& a, float b) { a = a + b; asm("" : "+v"(a)); }
DI void fmac_s(float& a, float b, float c) { a = __builtin_fmaf(b, c, a); asm("" : "+v"(a)); }
DI float mul_s(float a, float b) { float r = a * b; asm("" : "+v"(r)); return r; }
#define ST_MODE 0
DI void st16(void* p, u32x4 v) {
  *(u32x4*)p = v;
}
DI float lo16(unsigned u) { return __uint_as_float(u << 16); }
DI float hi16(unsigned u) { return __uint_as_float(u & 0xffff0000u); }

#define ST1(i)

constexpr int K1_AB = 8192;
constexpr int K1_SLOT = 16384;
constexpr int K1_NSLOT = 9;
constexpr int K1_LDS = K1_NSLOT * K1_SLOT;
constexpr int K1_XS = 0;
constexpr int K1_CSP = 256;
constexpr int K1_CST = K1_CSP + 16 * 128;
constexpr int K1_XP = K1_CST + 128;

DI void waitv(int N) {
  if (N >= 28) WAITV(28); else if (N >= 24) WAITV(24); else if (N >= 20) WAITV(20); else if (N >= 16) WAITV(16);
  else if (N >= 12) WAITV(12); else if (N >= 8) WAITV(8); else if (N >= 4) WAITV(4); else WAITV(0);
}

DI void k1_helper(char* lds, int panel, int col0, const u16* __restrict__ x, const u16* __restrict__ w) {
  const int lane = threadIdx.x & 63;
  const int hw = __builtin_amdgcn_readfirstlane(threadIdx.x >> 6) - 8;
  const u16* src[4];
#pragma unroll
  for (int i = 0; i < 2; ++i) {
    const int row = (hw + 4 * i) * 16 + (lane >> 2), ch = (lane & 3) ^ ((row >> 2) & 3);
    src[i] = x + (size_t)(panel * 128 + row) * D_ + ch * 8;
    const int kr = (hw + 4 * i) * 4 + (lane >> 4), pc = (lane & 15) ^ ((kr & 3) << 2);
    src[2 + i] = w + (size_t)kr * (3 * D_) + col0 + pc * 8;
  }
  auto stage = [&](int kc) {
    char* d = lds + (kc % K1_NSLOT) * K1_SLOT + hw * 1024;
    glds16(src[0] + kc * 32, d); glds16(src[1] + kc * 32, d + 4096);
    glds16(src[2] + (size_t)kc * 32 * (3 * D_), d + K1_AB); glds16(src[3] + (size_t)kc * 32 * (3 * D_), d + K1_AB + 4096);
  };
  const int g = lane >> 4, pc = lane & 15;
  const lds_cptr cr = (lds_cptr)lds + K1_AB + (8 * hw + g) * 256 + ((pc ^ (g << 2)) << 4);
  float cs[8];
#pragma unroll
  for (int c = 0; c < 8; ++c) cs[c] = 0.f;
  constexpr int ISS[17] = {3, 5, 7, 9, 11, 13, 14, 15, 16, 16, 16, 16, 16, 16, 16, 16, 16};
  stage(0); stage(1); stage(2);
  waitv(8);
  BAR();
#pragma unroll
  for (int kc = 0; kc < 16; ++kc) {
    waitv(kc >= 14 ? 0 : (ISS[kc] - kc - 2) * 4);
    BAR();
#pragma unroll
    for (int c = ISS[kc]; c < ISS[kc + 1]; ++c) stage(c);
    const u32x4 b0 = __builtin_bit_cast(u32x4, lds_read16(cr + (kc % K1_NSLOT) * K1_SLOT));
    const u32x4 b1 = __builtin_bit_cast(u32x4, lds_read16(cr + (kc % K1_NSLOT) * K1_SLOT + 1024));
#pragma unroll
    for (int j = 0; j < 4; ++j) {
      add_s(cs[2 * j], lo16(b0[j])); add_s(cs[2 * j + 1], hi16(b0[j]));
      add_s(cs[2 * j], lo16(b1[j])); add_s(cs[2 * j + 1], hi16(b1[j]));
    }
  }
  float* sc = (float*)lds;
  {
    f32x4 c0 = {cs[0], cs[1], cs[2], cs[3]}, c1 = {cs[4], cs[5], cs[6], cs[7]};
    float* d = sc + K1_CSP + (4 * hw + g) * 128 + pc * 8;
    *(f32x4*)d = c0; *(f32x4*)(d + 4) = c1;
  }
  __syncthreads();
  if (hw < 2) {
    const int i = hw * 64 + lane;
    float t = 0.f;
#pragma unroll
    for (int q = 0; q < 16; ++q) t += sc[K1_CSP + q * 128 + i];
    sc[K1_CST + i] = t;
  } else {
    const int i = (hw - 2) * 64 + lane;
    const float* p = sc + K1_XP + i;
    const float mean = ((p[0] + p[256]) + (p[512] + p[768])) * (1.f / D_);
    const float var = __builtin_fmaxf(((p[128] + p[384]) + (p[640] + p[896])) * (1.f / D_) - mean * mean, 0.f);
    sc[K1_XS + i] = mean; sc[K1_XS + 128 + i] = __builtin_amdgcn_rsqf(var + 1e-5f);
  }
  __syncthreads();
}

template <int TYPE, int CW>
DI void k1_gemm(char* lds, int panel, int st0, char* __restrict__ ws) {
  const int tid = threadIdx.x, lane = tid & 63, r = lane & 31, h = lane >> 5;
  const int mw = __builtin_amdgcn_readfirstlane(tid >> 6) & 1;
  f32x16 acc[2] = {f32x16{}, f32x16{}};
  const int arow = mw * 64 + r;
  const int aswz = (arow >> 2) & 3;
  const lds_cptr ab0 = (lds_cptr)lds + arow * 64 + ((h ^ aswz) << 4), ab1 = (lds_cptr)lds + arow * 64 + (((2 + h) ^ aswz) << 4);
  const lds_cptr bb = (lds_cptr)lds + K1_AB + (8 * h + ((lane & 15) >> 2)) * 256 + (CW ^ ((lane & 15) >> 2)) * 64 + ((lane >> 4) & 1) * 32 + (lane & 3) * 8;
  bf16x8 af[2][2]; s16x4 bl[2], bh[2];
  float sm[2][2] = {{0.f, 0.f}, {0.f, 0.f}}, sq[2][2] = {{0.f, 0.f}, {0.f, 0.f}};
#define K1_FRAGS(buf, soff, ks) do { __builtin_amdgcn_s_waitcnt(0xC07F); const lds_cptr a_ = ((ks) ? ab1 : ab0) + (soff); \
    af[buf][0] = lds_read16(a_); af[buf][1] = lds_read16(a_ + 2048); \
    bl[buf] = tr_read(bb + (soff) + (ks) * 4096); bh[buf] = tr_read(bb + (soff) + (ks) * 4096 + 1024); SB(); } while (0)
#define K1_MMA(buf) do { const bf16x8 bf = __builtin_shufflevector(bl[buf], bh[buf], 0, 1, 2, 3, 4, 5, 6, 7); \
    _Pragma("unroll") for (int mi = 0; mi < 2; ++mi) { \
      if (TYPE == 1) acc[mi] = MFMA(af[buf][mi], bf, acc[mi]); else acc[mi] = MFMA(bf, af[buf][mi], acc[mi]); \
      const unsigned u = __builtin_bit_cast(u32x4, af[buf][mi])[CW]; const float lo = lo16(u), hi = hi16(u); \
      add_s(sm[mi][0], lo); add_s(sm[mi][1], hi); fmac_s(sq[mi][0], lo, lo); fmac_s(sq[mi][1], hi, hi); \
      asm volatile("" : "+v"(sm[mi][0]), "+v"(sm[mi][1]), "+v"(sq[mi][0]), "+v"(sq[mi][1])); SB(); } } while (0)
  ST1(0);
  BAR();
  ST1(1);
  K1_FRAGS(0, 0, 0);
  int scur = 0, snext = K1_SLOT;
#pragma unroll 1
  for (int kc = 0; kc < 16; ++kc) {
    BAR();
    K1_FRAGS(1, scur, 1);
    K1_MMA(0);
    K1_FRAGS(0, snext, 0);
    K1_MMA(1);
    scur = snext; snext = (snext == (K1_NSLOT - 1) * K1_SLOT) ? 0 : snext + K1_SLOT;
  }
  ST1(17);
#pragma unroll
  for (int mi = 0; mi < 2; ++mi) {
    const float t = swap_add(sm[mi][0] + sm[mi][1]), u = swap_add(sq[mi][0] + sq[mi][1]);
    float* xp = (float*)lds + K1_XP + CW * 256 + mw * 64 + mi * 32 + r;
    if (h == 0) { xp[0] = t; xp[128] = u; }
  }
  __syncthreads();
  __syncthreads();
  const float* xs = (const float*)lds + K1_XS;
  const float* cp = (const float*)lds + K1_CST + CW * 32;
  const int st = st0 + CW;
#pragma unroll
  for (int mi = 0; mi < 2; ++mi) {
    const int mt = panel * 4 + mw * 2 + mi;
    const int lr = mw * 64 + mi * 32;
    if (TYPE == 0) {
      const float mean = xs[lr + r], rstd = xs[128 + lr + r];
      const float sc = (st < 16) ? rstd * (0.25f * 1.4426950408889634f) : rstd;
      char* dstp = ws + ((st < 16) ? WS_Q : WS_K) + ((size_t)((st & 15) * 2) * 64 + mt) * 1024 + lane * 16;
#pragma unroll
      for (int hd = 0; hd < 2; ++hd) {
        const f32x4 c0 = *(const f32x4*)(cp + 16 * hd + 4 * h), c1 = *(const f32x4*)(cp + 16 * hd + 8 + 4 * h);
        float v[8];
#pragma unroll
        for (int j = 0; j < 4; ++j) { v[j] = (acc[mi][8 * hd + j] - mean * c0[j]) * sc; v[4 + j] = (acc[mi][8 * hd + 4 + j] - mean * c1[j]) * sc; }
        u32x4 o = {pk2(v[0], v[1]), pk2(v[2], v[3]), pk2(v[4], v[5]), pk2(v[6], v[7])};
        st16(dstp + (size_t)hd * 64 * 1024, o);
      }
    } else {
      const float c = cp[r];
      char* dstp = ws + WS_V + ((size_t)((st - 32) * 2 + (r >> 4)) * 64 + mt) * 1024 + h * 256 + (r & 15) * 16;
#pragma unroll
      for (int ks = 0; ks < 2; ++ks) {
        const f32x4 m0 = *(const f32x4*)(xs + lr + 16 * ks + 4 * h), m1 = *(const f32x4*)(xs + lr + 16 * ks + 8 + 4 * h);
        const f32x4 r0 = *(const f32x4*)(xs + 128 + lr + 16 * ks + 4 * h), r1 = *(const f32x4*)(xs + 128 + lr + 16 * ks + 8 + 4 * h);
        float v[8];
#pragma unroll
        for (int j = 0; j < 4; ++j) { v[j] = (acc[mi][8 * ks + j] - m0[j] * c) * r0[j]; v[4 + j] = (acc[mi][8 * ks + 4 + j] - m1[j] * c) * r1[j]; }
        u32x4 o = {pk2(v[0], v[1]), pk2(v[2], v[3]), pk2(v[4], v[5]), pk2(v[6], v[7])};
        st16(dstp + ks * 512, o);
      }
    }
  }
  ST1(18);
}

__global__ __launch_bounds__(768, 3) void k1_kernel(const u16* __restrict__ x, const u16* __restrict__ wqkv, const u16* __restrict__ wo,
                                                    const u16* __restrict__ wg, const u16* __restrict__ wu, const u16* __restrict__ wd,
                                                    char* __restrict__ ws) {
  __shared__ __attribute__((aligned(16))) char lds[K1_LDS];
  const int bid = blockIdx.x;
  const int xcd = bid & 7, idx = bid >> 3;
  const int wave = __builtin_amdgcn_readfirstlane(threadIdx.x >> 6);
  const int lane = threadIdx.x & 63, c = lane & 31, h = lane >> 5;
  if (idx < 24) {
    const int panel = (xcd & 3) * 4 + (idx & 3), grp = (xcd >> 2) * 6 + (idx >> 2);
    if (wave >= 8) k1_helper(lds, panel, grp * 128, x, wqkv);
    else if (grp < 8) {
      switch (wave >> 1) {
        case 0: k1_gemm<0, 0>(lds, panel, grp * 4, ws); break; case 1: k1_gemm<0, 1>(lds, panel, grp * 4, ws); break;
        case 2: k1_gemm<0, 2>(lds, panel, grp * 4, ws); break; default: k1_gemm<0, 3>(lds, panel, grp * 4, ws); break;
      }
    } else {
      switch (wave >> 1) {
        case 0: k1_gemm<1, 0>(lds, panel, grp * 4, ws); break; case 1: k1_gemm<1, 1>(lds, panel, grp * 4, ws); break;
        case 2: k1_gemm<1, 2>(lds, panel, grp * 4, ws); break; default: k1_gemm<1, 3>(lds, panel, grp * 4, ws); break;
      }
    }
  } else {
    const int pb = (idx - 24) * 8 + xcd;
    if (wave < 4) {
      u32x4 v[8];
#pragma unroll
      for (int i = 0; i < 8; ++i) v[i] = *(const u32x4*)(x + (size_t)(pb * 32 + wave * 8 + i) * D_ + lane * 8);
      float f[8][8], sm[8];
#pragma unroll
      for (int i = 0; i < 8; ++i) {
#pragma unroll
        for (int j = 0; j < 4; ++j) { f[i][2 * j] = lo16(v[i][j]); f[i][2 * j + 1] = hi16(v[i][j]); }
        sm[i] = ((f[i][0] + f[i][1]) + (f[i][2] + f[i][3])) + ((f[i][4] + f[i][5]) + (f[i][6] + f[i][7]));
      }
#pragma unroll
      for (int d = 1; d < 64; d <<= 1)
#pragma unroll
        for (int i = 0; i < 8; ++i) sm[i] += __shfl_xor(sm[i], d);
#pragma unroll
      for (int i = 0; i < 8; ++i) {
        const float mean = sm[i] * (1.f / D_);
        float q = 0.f;
#pragma unroll
        for (int j = 0; j < 8; ++j) { f[i][j] -= mean; q += f[i][j] * f[i][j]; }
        sm[i] = q;
      }
#pragma unroll
      for (int d = 1; d < 64; d <<= 1)
#pragma unroll
        for (int i = 0; i < 8; ++i) sm[i] += __shfl_xor(sm[i], d);
#pragma unroll
      for (int i = 0; i < 8; ++i) {
        const float rstd = __builtin_amdgcn_rsqf(sm[i] * (1.f / D_) + 1e-5f);
        u32x4 o;
#pragma unroll
        for (int j = 0; j < 4; ++j) o[j] = pk2(f[i][2 * j] * rstd, f[i][2 * j + 1] * rstd);
        *(u32x4*)(ws + WS_X + ((size_t)pb * 32 + (lane >> 1)) * 1024 + ((lane & 1) * 32 + wave * 8 + i) * 16) = o;
      }
    } else {
      constexpr int NG = 86 * 32, NF = NG + KST3 * 16;
      const int wv = pb * 8 + wave - 4;
      char* wl = lds + wave * 10240;
      const int kr = lane >> 2, p = lane & 3;
#pragma unroll
      for (int i = 0; i < 10; ++i) {
        int f = wv + i * 512; if (f > NF - 1) f = NF - 1;
        const u16* src;
        if (f < NG) { const int st = f >> 5, ks = f & 31; src = ((p < 2) ? wg : wu) + (size_t)(ks * 16 + kr) * F_ + st * 16 + (p & 1) * 8; }
        else { const int g = f - NG, ks = g >> 4, nt = g & 15; src = ((ks < 32) ? wo + (size_t)(ks * 16 + kr) * D_ : wd + (size_t)((ks - 32) * 16 + kr) * D_) + nt * 32 + p * 8; }
        glds16(src, wl + i * 1024);
      }
      WAITV(0);
      const lds_cptr tb = (lds_cptr)wl + ((lane & 15) >> 2) * 64 + ((lane >> 4) & 1) * 32 + (lane & 3) * 8;
#pragma unroll
      for (int i = 0; i < 10; ++i) {
        const int f = wv + i * 512;
        const bool nat = f < NG;
        const s16x4 lo = tr_read(tb + i * 1024 + (nat ? 8 * h : 4 * h) * 64), hi = tr_read(tb + i * 1024 + (nat ? 8 * h + 4 : 8 + 4 * h) * 64);
        const bf16x8 o = __builtin_shufflevector(lo, hi, 0, 1, 2, 3, 4, 5, 6, 7);
        char* d = nat ? ws + WS_G + (size_t)f * 1024 : ws + WS_W + (size_t)(f - NG) * 1024;
        if (f < NF) st16(d + lane * 16, __builtin_bit_cast(u32x4, o));
      }
    }
  }
}

constexpr int K2_V = 65536, K2_C = 131072, K2_Q = K2_C + 8192, K2_CNT = K2_Q + 8192, K2_LDS = K2_CNT + 64;
#define K2_HEAD 102.f
#define K2_INIT 8
constexpr float HEAD = K2_HEAD;
#define K2_ITERS 16

DI float cexp2(float x) { return __builtin_amdgcn_fmed3f(__builtin_amdgcn_exp2f(x), 0.f, 1.f); }
DI void exp_pack(const f32x16& s, u32x4& p0, u32x4& p1) {
#pragma unroll
  for (int i = 0; i < 4; ++i) { p0[i] = pk2(cexp2(s[2 * i]), cexp2(s[2 * i + 1])); p1[i] = pk2(cexp2(s[8 + 2 * i]), cexp2(s[9 + 2 * i])); }
}
DI float max16(const f32x16& s) {
  float m = __builtin_fmaxf(__builtin_fmaxf(s[0], s[1]), s[2]);
#pragma unroll
  for (int i = 3; i < 15; i += 2) m = __builtin_fmaxf(__builtin_fmaxf(m, s[i]), s[i + 1]);
  return __builtin_fmaxf(m, s[15]);
}
DI f32x16 splat16(float v) { f32x16 r;
#pragma unroll
  for (int i = 0; i < 16; ++i) r[i] = v;
  return r; }

DI void k2_fix(f32x16& o, float& nm, const bf16x8 qf, lds_cptr kp0, lds_cptr vp0, int vinc, int t0, int h) {
  float sm = -3.0e38f;
#pragma unroll 1
  for (int t = t0; t < t0 + 4; ++t) {
    const f32x16 s = MFMA(lds_read16(kp0 + t * 1024), qf, splat16(nm));
    sm = __builtin_fmaxf(sm, max16(s));
    u32x4 p0, p1; exp_pack(s, p0, p1);
#pragma unroll
    for (int i = 0; i < 4; ++i) { p0[i] ^= 0x80008000u; p1[i] ^= 0x80008000u; }
    o = MFMA(lds_read16(vp0 + t * vinc), BC(p0), o);
    o = MFMA(lds_read16(vp0 + t * vinc + 512), BC(p1), o);
  }
  sm = swap_max(sm);
  const float lo = swap_add(h == 0 ? o[8] : 0.f);
  sm = __builtin_fmaxf(sm, __builtin_amdgcn_logf(__builtin_fmaxf(lo, 1e-37f)));
  const float dl = __builtin_fmaxf(sm + HEAD, 0.f);
  const float f = __builtin_amdgcn_exp2f(-dl);
#pragma unroll
  for (int i = 0; i < 16; ++i) o[i] *= f;
  nm -= dl;
#pragma unroll 1
  for (int t = t0; t < t0 + 4; ++t) {
    const f32x16 s = MFMA(lds_read16(kp0 + t * 1024), qf, splat16(nm));
    u32x4 p0, p1; exp_pack(s, p0, p1);
    o = MFMA(lds_read16(vp0 + t * vinc), BC(p0), o);
    o = MFMA(lds_read16(vp0 + t * vinc + 512), BC(p1), o);
  }
}
DI void k2_sync(char* lds, int target) {
  volatile __attribute__((address_space(3))) unsigned* cnt = (volatile __attribute__((address_space(3))) unsigned*)(lds + K2_CNT);
  if ((threadIdx.x & 63) == 0) __hip_atomic_fetch_add((__attribute__((address_space(3))) unsigned*)(lds + K2_CNT), 1u, __ATOMIC_RELAXED, __HIP_MEMORY_SCOPE_WORKGROUP);
  while ((int)__builtin_amdgcn_readfirstlane(*cnt) < target) __builtin_amdgcn_s_sleep(1);
  asm volatile("" ::: "memory");
}

DI void k2_attn(char* lds, char* __restrict__ ws) {
  const int tid = threadIdx.x, lane = tid & 63, r = lane & 31, h = lane >> 5;
  const int wave = __builtin_amdgcn_readfirstlane(tid >> 6);
  const int bid = blockIdx.x;
  const int head = (bid & 7) * 4 + ((bid >> 3) & 3), qb = bid >> 5;
  const int mt = qb * 8 + wave;
  const char* kg = ws + WS_K + (size_t)head * 65536 + wave * 1024 + lane * 16;
  const char* vg = ws + WS_V + (size_t)head * 65536 + wave * 1024 + lane * 16;
#define K2_STAGE(c) do { glds16(kg + (c) * 8192, lds + (c) * 8192 + wave * 1024); glds16(vg + (c) * 8192, lds + K2_V + (c) * 8192 + wave * 1024); } while (0)
  glds16(ws + WS_Q + ((size_t)head * 64 + mt) * 1024 + lane * 16, lds + K2_Q + wave * 1024);
  K2_STAGE(0); K2_STAGE(1);
  {
    const unsigned c = ((tid & 31) == 0) ? 0x3f803f80u : 0u;
    u32x4 z = {c, c, c, c}; *(u32x4*)(lds + K2_C + tid * 16) = z;
  }
  asm volatile("s_waitcnt vmcnt(3) lgkmcnt(0)" ::: "memory");
  k2_sync(lds, 8);
  const bf16x8 qf = lds_read16((lds_cptr)lds + K2_Q + wave * 1024 + lane * 16);
  const lds_cptr kp0 = (lds_cptr)lds + lane * 16;
  const lds_cptr vp0 = (r < 16) ? (lds_cptr)lds + K2_V + h * 256 + r * 16 : (lds_cptr)lds + K2_C + (r == 16 ? 0 : 16);
  const int vinc = (r < 16) ? 1024 : 0;
  f32x16 o = f32x16{};
  float nm;
  {
    const f32x16 z = f32x16{};
    float m = max16(MFMA(lds_read16(kp0), qf, z));
#pragma unroll
    for (int t = 1; t < K2_INIT; ++t) m = __builtin_fmaxf(m, max16(MFMA(lds_read16(kp0 + t * 1024), qf, z)));
    nm = -(swap_max(m) + HEAD);
  }
  K2_STAGE(2); K2_STAGE(3);
  WAITV(4); k2_sync(lds, 16);
#define EXP4(S, b) do { _Pragma("unroll") for (int i = 0; i < 4; ++i) e[i] = cexp2(S[b + i]); PIN(e); SB(); } while (0)
#define EXP4P(P, S, b) do { P[0] = pk2(e[0], e[1]); P[1] = pk2(e[2], e[3]); \
    P[2] = pk2(cexp2(S[b]), cexp2(S[b + 1])); P[3] = pk2(cexp2(S[b + 2]), cexp2(S[b + 3])); PIN(P); SB(); } while (0)
#define THEAD(j, SC, SN, KN, KL, VL0, VL1) do { \
    EXP4(SC, 0); \
    SN = MFMA(KN, qf, SN); PIN(SN); SB(); \
    KL = lds_read16(kp + (j + 2) * 1024); VL0 = lds_read16(vp + (j + 1) * 1024); VL1 = lds_read16(vp + (j + 1) * 1024 + 512); SB(); \
    EXP4P(p0, SC, 4); } while (0)
#define TTAIL(SC, SN, KO, VU0, VU1) do { \
    o = MFMA(VU0, BC(p0), o); PIN(o); SB(); \
    EXP4(SC, 8); \
    SN = MFMA(KO, nqf, SN); PIN(SN); SB(); \
    EXP4P(p1, SC, 12); \
    o = MFMA(VU1, BC(p1), o); PIN(o); SB(); } while (0)
  f32x4 e;
  bf16x8 nqf;
  { u32x4 t = __builtin_bit_cast(u32x4, qf); t[0] ^= 0x80008000u; t[1] ^= 0x80008000u; t[2] ^= 0x80008000u; t[3] ^= 0x80008000u; nqf = BC(t); }
  int it = 0;
#pragma unroll 1
  for (;;) {
    lds_cptr kp = kp0 + it * 4096, vp = vp0 + it * 4 * vinc;
    bf16x8 k0 = lds_read16(kp), k1 = lds_read16(kp + 1024), k2, k3 = k0;
    bf16x8 vA0 = lds_read16(vp), vA1 = lds_read16(vp + 512), vB0, vB1;
    f32x16 sA = MFMA(k0, qf, splat16(nm)), sB = sA;
    u32x4 p0, p1;
    THEAD(0, sA, sB, k1, k2, vB0, vB1);
    bool trig;
#pragma unroll 1
    do {
      TTAIL(sA, sB, k3, vA0, vA1);
      THEAD(1, sB, sA, k2, k3, vA0, vA1); TTAIL(sB, sA, k0, vB0, vB1);
      THEAD(2, sA, sB, k3, k0, vB0, vB1); TTAIL(sA, sB, k1, vA0, vA1);
      THEAD(3, sB, sA, k0, k1, vA0, vA1); TTAIL(sB, sA, k2, vB0, vB1);
      kp += 4096; vp += 4 * vinc; ++it;
      if (it <= 2) {
        if (it == 1) { K2_STAGE(4); K2_STAGE(5); }
        else { K2_STAGE(6); K2_STAGE(7); WAITV(8); k2_sync(lds, 24); }
      }
      if (it == 6) { WAITV(0); k2_sync(lds, 32); }
      THEAD(0, sA, sB, k1, k2, vB0, vB1);
      trig = __any(o[8] >= 1.f);
    } while (it < K2_ITERS && !trig);
    if (__builtin_expect(!trig, 1)) break;
    k2_fix(o, nm, qf, kp0, vp0, vinc, 4 * it - 4, h);
    if (it >= K2_ITERS) break;
  }
  asm volatile("s_nop 15" : "+v"(o));
  const float il = 1.f / swap_add(h == 0 ? o[8] : 0.f);
  u32x4 ov = {pk2(mul_s(o[0], il), mul_s(o[1], il)), pk2(mul_s(o[2], il), mul_s(o[3], il)), pk2(mul_s(o[4], il), mul_s(o[5], il)), pk2(mul_s(o[6], il), mul_s(o[7], il))};
  st16(ws + WS_A + ((size_t)mt * KST3 + head) * 1024 + lane * 16, ov);
}

DI void k2_ffn(char* __restrict__ ws) {
  const int lane = threadIdx.x & 63;
  const int fw = __builtin_amdgcn_readfirstlane(threadIdx.x >> 6) - 8;
  const int bid = blockIdx.x;
  const int sp = (bid & 7) + 8 * fw, mp = bid >> 3;
  if (sp < 43) {
    const __amdgpu_buffer_rsrc_t rs = __builtin_amdgcn_make_buffer_rsrc(ws, 0, 0x7fffffff, 0x00020000);
    const int xo = (int)WS_X + mp * 65536, wo = (int)WS_G + sp * 65536, l16 = lane * 16;
    f32x16 acc[2][2] = {{f32x16{}, f32x16{}}, {f32x16{}, f32x16{}}};
#define FF_PF 3
    constexpr int PF = FF_PF;
    u32x4 a0[PF], a1[PF], b0[PF], b1[PF];
#define BLD(off) __builtin_amdgcn_raw_buffer_load_b128(rs, l16, (off), 0)
#define FF_LOAD(i, k) do { a0[i] = BLD(xo + (k) * 1024); a1[i] = BLD(xo + 32768 + (k) * 1024); \
    b0[i] = BLD(wo + (k) * 1024); b1[i] = BLD(wo + 32768 + (k) * 1024); } while (0)
#pragma unroll
    for (int i = 0; i < PF; ++i) FF_LOAD(i, i);
#pragma unroll
    for (int k = 0; k < 32; ++k) {
      const int i = k % PF;
      acc[0][0] = MFMA(BC(b0[i]), BC(a0[i]), acc[0][0]); acc[0][1] = MFMA(BC(b1[i]), BC(a0[i]), acc[0][1]);
      acc[1][0] = MFMA(BC(b0[i]), BC(a1[i]), acc[1][0]); acc[1][1] = MFMA(BC(b1[i]), BC(a1[i]), acc[1][1]);
      PIN(acc[1][1]); SB();
      if (k + PF < 32) FF_LOAD(i, k + PF);
      SB();
    }
    asm volatile("s_nop 15\n\ts_nop 15" : "+v"(acc[0][0]), "+v"(acc[0][1]), "+v"(acc[1][0]), "+v"(acc[1][1]));
    SB();
#pragma unroll
    for (int mi = 0; mi < 2; ++mi)
#pragma unroll
      for (int s = 0; s < 2; ++s) {
        float v[8];
#pragma unroll
        for (int j = 0; j < 8; ++j) {
          const float g = acc[mi][s][j], u = acc[mi][s][8 + j];
          float d = __builtin_amdgcn_exp2f(mul_s(g, -1.4426950408889634f)); add_s(d, 1.f);
          v[j] = mul_s(mul_s(g, __builtin_amdgcn_rcpf(d)), u);
        }
        u32x4 o = {pk2(v[0], v[1]), pk2(v[2], v[3]), pk2(v[4], v[5]), pk2(v[6], v[7])};
        st16(ws + WS_A + ((size_t)(mp * 2 + mi) * KST3 + 32 + sp * 2 + s) * 1024 + lane * 16, o);
      }
  }
}

__global__ __launch_bounds__(1024, 4) void k2_kernel(char* __restrict__ ws) {
  __shared__ __attribute__((aligned(16))) char lds[K2_LDS];
  if (threadIdx.x == 0) *(unsigned*)(lds + K2_CNT) = 0u;
  __syncthreads();
  if (__builtin_amdgcn_readfirstlane(threadIdx.x) < 512) k2_attn(lds, ws); else k2_ffn(ws);
}

constexpr int K3_ROWB = 64 * 4 + 16;
__global__ __launch_bounds__(256, 1) void k3_kernel(const u16* __restrict__ x, const char* __restrict__ ws, u16* __restrict__ out) {
  __shared__ __attribute__((aligned(16))) char lds[4 * 64 * K3_ROWB];
  const int tid = threadIdx.x, lane = tid & 63, r = lane & 31, h = lane >> 5;
  const int wave = __builtin_amdgcn_readfirstlane(tid >> 6);
  const int bid = blockIdx.x;
  const int mb = (bid & 7) * 4 + ((bid >> 3) & 3), nb = bid >> 5;
  const int k0 = wave * 30 - (wave > 2 ? 1 : 0) , nk = (wave < 2) ? 30 : 29;
  const char* ap = ws + WS_A + ((size_t)(mb * 2) * KST3 + k0) * 1024 + lane * 16;
  const char* wp = ws + WS_W + ((size_t)k0 * 16 + nb * 2) * 1024 + lane * 16;
  f32x16 acc[2][2];
#pragma unroll
  for (int i = 0; i < 2; ++i)
#pragma unroll
    for (int j = 0; j < 2; ++j) acc[i][j] = f32x16{};
  constexpr int PF = 12;
  u32x4 a0[PF], a1[PF], b0[PF], b1[PF];
  const int klast = nk - 1;
#define K3_LOAD(i, k) do { const int kk_ = ((k) < 29) ? (k) : klast; \
    a0[i] = *(const u32x4*)(ap + kk_ * 1024); a1[i] = *(const u32x4*)(ap + (size_t)KST3 * 1024 + kk_ * 1024); \
    b0[i] = *(const u32x4*)(wp + kk_ * 16384); b1[i] = *(const u32x4*)(wp + kk_ * 16384 + 1024); } while (0)
#pragma unroll
  for (int i = 0; i < PF; ++i) K3_LOAD(i, i);
#pragma unroll
  for (int k = 0; k < 30; ++k) {
    const int i = k % PF;
    if (k < 29 || nk == 30) {
      acc[0][0] = MFMA(BC(b0[i]), BC(a0[i]), acc[0][0]); acc[0][1] = MFMA(BC(b1[i]), BC(a0[i]), acc[0][1]);
      acc[1][0] = MFMA(BC(b0[i]), BC(a1[i]), acc[1][0]); acc[1][1] = MFMA(BC(b1[i]), BC(a1[i]), acc[1][1]);
    }
    if (k + PF < 30) K3_LOAD(i, k + PF);
    __builtin_amdgcn_sched_barrier(0);
  }
  char* pw = lds + wave * 64 * K3_ROWB;
#pragma unroll
  for (int i = 0; i < 2; ++i)
#pragma unroll
    for (int j = 0; j < 2; ++j)
#pragma unroll
      for (int g = 0; g < 4; ++g) {
        f32x4 v = {acc[i][j][4 * g], acc[i][j][4 * g + 1], acc[i][j][4 * g + 2], acc[i][j][4 * g + 3]};
        *(f32x4*)(pw + (i * 32 + r) * K3_ROWB + (j * 32 + 8 * g + 4 * h) * 4) = v;
      }
  __syncthreads();
  const int m = tid >> 2, n0 = (tid & 3) * 16;
  const size_t go = (size_t)(mb * 64 + m) * D_ + nb * 64 + n0;
  const u32x4 x0 = *(const u32x4*)(x + go), x1 = *(const u32x4*)(x + go + 8);
  float s[16];
#pragma unroll
  for (int j = 0; j < 4; ++j) {
    s[2 * j] = __uint_as_float(x0[j] << 16); s[2 * j + 1] = __uint_as_float(x0[j] & 0xffff0000u);
    s[8 + 2 * j] = __uint_as_float(x1[j] << 16); s[8 + 2 * j + 1] = __uint_as_float(x1[j] & 0xffff0000u);
  }
#pragma unroll
  for (int w = 0; w < 4; ++w)
#pragma unroll
    for (int q = 0; q < 4; ++q) {
      const f32x4 v = *(const f32x4*)(lds + w * 64 * K3_ROWB + m * K3_ROWB + (n0 + 4 * q) * 4);
#pragma unroll
      for (int j = 0; j < 4; ++j) s[4 * q + j] += v[j];
    }
  u32x4 o0 = {pk2(s[0], s[1]), pk2(s[2], s[3]), pk2(s[4], s[5]), pk2(s[6], s[7])};
  u32x4 o1 = {pk2(s[8], s[9]), pk2(s[10], s[11]), pk2(s[12], s[13]), pk2(s[14], s[15])};
  *(u32x4*)(out + go) = o0; *(u32x4*)(out + go + 8) = o1;
}

extern "C" void kernel_launch(void* const* d_in, const int* in_sizes, int n_in, void* d_out, int out_size, void* d_ws, size_t ws_size,
                              hipStream_t stream) {
  const u16* x = (const u16*)d_in[0];
  const u16* wqkv = (const u16*)d_in[1];
  const u16* wo = (const u16*)d_in[2];
  const u16* wg = (const u16*)d_in[4];
  const u16* wu = (const u16*)d_in[5];
  const u16* wd = (const u16*)d_in[6];
  char* ws = (char*)d_ws;
  k1_kernel<<<256, 768, 0, stream>>>(x, wqkv, wo, wg, wu, wd, ws);
  k2_kernel<<<256, 1024, 0, stream>>>(ws);
  k3_kernel<<<256, 256, 0, stream>>>(x, ws, (u16*)d_out);
}
```

```cpp
#include <hip/hip_runtime.h>
#include <hip/hip_bf16.h>

typedef short bf16x8 __attribute__((ext_vector_type(8)));
typedef short s16x4 __attribute__((ext_vector_type(4)));
typedef float f32x16 __attribute__((ext_vector_type(16)));
typedef float f32x4 __attribute__((ext_vector_type(4)));
typedef unsigned u32x4 __attribute__((ext_vector_type(4)));
typedef unsigned u32x2 __attribute__((ext_vector_type(2)));
typedef unsigned short u16;
typedef __attribute__((address_space(3))) const char* lds_cptr;
typedef __bf16 bf16x2_t __attribute__((ext_vector_type(2)));
typedef float f32x2_t __attribute__((ext_vector_type(2)));

#define DI __device__ __forceinline__
#define MFMA(a, b, c) __builtin_amdgcn_mfma_f32_32x32x16_bf16((a), (b), (c), 0, 0, 0)
#define BC(x) __builtin_bit_cast(bf16x8, x)
#define WAITV(n) asm volatile("s_waitcnt vmcnt(" #n ")" ::: "memory")
#define BAR() __builtin_amdgcn_s_barrier()
#define SB() __builtin_amdgcn_sched_barrier(0)
#define PIN(x) asm volatile("" : "+v"(x))

constexpr int S_ = 2048, D_ = 512, F_ = 1376, NH = 32;
constexpr int KST3 = 118;
constexpr size_t WS_Q = 0;
constexpr size_t WS_K = WS_Q + (size_t)NH * 64 * 1024;
constexpr size_t WS_V = WS_K + (size_t)NH * 64 * 1024;
constexpr size_t WS_A = WS_V + (size_t)NH * 64 * 1024;
constexpr size_t WS_W = WS_A + (size_t)64 * KST3 * 1024;
constexpr size_t WS_X = WS_W + (size_t)KST3 * 16 * 1024;
constexpr size_t WS_G = WS_X + (size_t)64 * 32 * 1024;

DI unsigned pk2(float a, float b) { f32x2_t v = {a, b}; return __builtin_bit_cast(unsigned, __builtin_convertvector(v, bf16x2_t)); }
DI float swap_add(float v) {
  auto rr = __builtin_amdgcn_permlane32_swap(__float_as_uint(v), __float_as_uint(v), false, false);
  return __uint_as_float(rr[0]) + __uint_as_float(rr[1]);
}
DI float swap_max(float v) {
  auto rr = __builtin_amdgcn_permlane32_swap(__float_as_uint(v), __float_as_uint(v), false, false);
  return __builtin_fmaxf(__uint_as_float(rr[0]), __uint_as_float(rr[1]));
}
DI s16x4 tr_read(lds_cptr p) {
  typedef short v4i16_t __attribute__((ext_vector_type(4)));
  return __builtin_bit_cast(s16x4, __builtin_amdgcn_ds_read_tr16_b64_v4i16((__attribute__((address_space(3))) v4i16_t*)p));
}
DI bf16x8 lds_read16(lds_cptr p) { return *(const __attribute__((address_space(3))) bf16x8*)p; }
DI void glds16(const void* g, char* lds) {
  __builtin_amdgcn_global_load_lds((const __attribute__((address_space(1))) void*)g, (__attribute__((address_space(3))) void*)lds, 16, 0, 0);
}
DI void add_s(float& a, float b) { a = a + b; asm("" : "+v"(a)); }
DI void fmac_s(float& a, float b, float c) { a = __builtin_fmaf(b, c, a); asm("" : "+v"(a)); }
DI float mul_s(float a, float b) { float r = a * b; asm("" : "+v"(r)); return r; }
#define ST_MODE 0
DI void st16(void* p, u32x4 v) {
  *(u32x4*)p = v;
}
DI float lo16(unsigned u) { return __uint_as_float(u << 16); }
DI float hi16(unsigned u) { return __uint_as_float(u & 0xffff0000u); }

#define ST1(i)

constexpr int K1_AB = 16384;
constexpr int K1_SLOT = 32768;
constexpr int K1_NSLOT = 4;
constexpr int K1_LDS = K1_NSLOT * K1_SLOT;
constexpr int K1_XS = 0;
constexpr int K1_CSP = 256;
constexpr int K1_CST = K1_CSP + 16 * 128;
constexpr int K1_XP = K1_CST + 128;

DI void k1_helper(char* lds, int panel, int col0, int rot, const u16* __restrict__ x, const u16* __restrict__ w) {
  const int lane = threadIdx.x & 63;
  const int hw = __builtin_amdgcn_readfirstlane(threadIdx.x >> 6) - 8;
  const u16 *sa, *sb;
  {
    const int row = hw * 32 + (lane >> 3), ch = (lane & 7) ^ ((row >> 1) & 3);
    sa = x + (size_t)(panel * 128 + row) * D_ + ch * 8;
    const int kr = hw * 16 + (lane >> 4), pc = (lane & 15) ^ ((kr & 3) << 2);
    sb = w + (size_t)kr * (3 * D_) + col0 + pc * 8;
  }
  const int a4 = ((lane & 7) ^ (((lane >> 4) & 3) | 4)) * 8 - ((lane & 7) ^ ((lane >> 4) & 3)) * 8;
  auto stage = [&](int kc) {
    char* d = lds + (kc % K1_NSLOT) * K1_SLOT + hw * 4096;
    const int k0 = ((kc + rot) & 7) * 64;
#pragma unroll
    for (int i = 0; i < 4; ++i) glds16(sa + k0 + i * 8 * D_ + ((i & 1) ? a4 : 0), d + i * 1024);
#pragma unroll
    for (int i = 0; i < 4; ++i) glds16(sb + (size_t)(k0 + i * 4) * (3 * D_), d + K1_AB + i * 1024);
  };
  const int g = lane >> 4, pc = lane & 15;
  const lds_cptr cr = (lds_cptr)lds + K1_AB + (16 * hw + g) * 256 + ((pc ^ (g << 2)) << 4);
  float cs[8];
#pragma unroll
  for (int c = 0; c < 8; ++c) cs[c] = 0.f;
  stage(0); stage(1);
  WAITV(8);
  BAR();
  stage(2);
#pragma unroll 1
  for (int kc = 0; kc < 8; ++kc) {
    if (kc < 6) WAITV(8); else WAITV(0);
    BAR();
    if (kc + 3 < 8) stage(kc + 3);
    u32x4 b[4];
#pragma unroll
    for (int i = 0; i < 4; ++i) b[i] = __builtin_bit_cast(u32x4, lds_read16(cr + (kc % K1_NSLOT) * K1_SLOT + i * 1024));
#pragma unroll
    for (int i = 0; i < 4; ++i)
#pragma unroll
      for (int j = 0; j < 4; ++j) { add_s(cs[2 * j], lo16(b[i][j])); add_s(cs[2 * j + 1], hi16(b[i][j])); }
  }
  float* sc = (float*)lds;
  {
    f32x4 c0 = {cs[0], cs[1], cs[2], cs[3]}, c1 = {cs[4], cs[5], cs[6], cs[7]};
    float* d = sc + K1_CSP + (4 * hw + g) * 128 + pc * 8;
    *(f32x4*)d = c0; *(f32x4*)(d + 4) = c1;
  }
  __syncthreads();
  if (hw < 2) {
    const int i = hw * 64 + lane;
    float t = 0.f;
#pragma unroll
    for (int q = 0; q < 16; ++q) t += sc[K1_CSP + q * 128 + i];
    sc[K1_CST + i] = t;
  } else {
    const int i = (hw - 2) * 64 + lane;
    const float* p = sc + K1_XP + i;
    const float mean = ((p[0] + p[256]) + (p[512] + p[768])) * (1.f / D_);
    const float var = __builtin_fmaxf(((p[128] + p[384]) + (p[640] + p[896])) * (1.f / D_) - mean * mean, 0.f);
    sc[K1_XS + i] = mean; sc[K1_XS + 128 + i] = __builtin_amdgcn_rsqf(var + 1e-5f);
  }
  __syncthreads();
}

template <int TYPE, int CW>
DI void k1_gemm(char* lds, int panel, int st0, char* __restrict__ ws) {
  const int tid = threadIdx.x, lane = tid & 63, r = lane & 31, h = lane >> 5;
  const int mw = __builtin_amdgcn_readfirstlane(tid >> 6) & 1;
  f32x16 acc[2] = {f32x16{}, f32x16{}};
  const int arow = mw * 64 + r;
  const int aswz = (arow >> 1) & 7;
  lds_cptr ab[4];
#pragma unroll
  for (int ks = 0; ks < 4; ++ks) ab[ks] = (lds_cptr)lds + arow * 128 + (((2 * ks + h) ^ aswz) << 4);
  const lds_cptr bb = (lds_cptr)lds + K1_AB + (8 * h + ((lane & 15) >> 2)) * 256 + (CW ^ ((lane & 15) >> 2)) * 64 + ((lane >> 4) & 1) * 32 + (lane & 3) * 8;
  bf16x8 af[2][4][2]; s16x4 bl[2][4], bh[2][4];
  float sm[2][2] = {{0.f, 0.f}, {0.f, 0.f}}, sq[2][2] = {{0.f, 0.f}, {0.f, 0.f}};
#define K1_FRAGS(buf, soff) do { __builtin_amdgcn_s_waitcnt(0xC07F); \
    _Pragma("unroll") for (int ks = 0; ks < 4; ++ks) { const lds_cptr a_ = ab[ks] + (soff); \
      af[buf][ks][0] = lds_read16(a_); af[buf][ks][1] = lds_read16(a_ + 4096); \
      bl[buf][ks] = tr_read(bb + (soff) + ks * 4096); bh[buf][ks] = tr_read(bb + (soff) + ks * 4096 + 1024); } SB(); } while (0)
#define K1_MMA(buf) do { _Pragma("unroll") for (int ks = 0; ks < 4; ++ks) { \
    const bf16x8 bf = __builtin_shufflevector(bl[buf][ks], bh[buf][ks], 0, 1, 2, 3, 4, 5, 6, 7); \
    _Pragma("unroll") for (int mi = 0; mi < 2; ++mi) { \
      if (TYPE == 1) acc[mi] = MFMA(af[buf][ks][mi], bf, acc[mi]); else acc[mi] = MFMA(bf, af[buf][ks][mi], acc[mi]); \
      const unsigned u = __builtin_bit_cast(u32x4, af[buf][ks][mi])[CW]; const float lo = lo16(u), hi = hi16(u); \
      add_s(sm[mi][0], lo); add_s(sm[mi][1], hi); fmac_s(sq[mi][0], lo, lo); fmac_s(sq[mi][1], hi, hi); \
      asm volatile("" : "+v"(sm[mi][0]), "+v"(sm[mi][1]), "+v"(sq[mi][0]), "+v"(sq[mi][1])); SB(); } } } while (0)
  ST1(0);
  BAR();
  ST1(1);
  K1_FRAGS(0, 0);
#pragma unroll 1
  for (int kc = 0; kc < 8; kc += 2) {
    BAR();
    K1_FRAGS(1, ((kc + 1) & 3) * K1_SLOT); K1_MMA(0);
    BAR();
    K1_FRAGS(0, ((kc + 2) & 3) * K1_SLOT); K1_MMA(1);
  }
  ST1(17);
#pragma unroll
  for (int mi = 0; mi < 2; ++mi) {
    const float t = swap_add(sm[mi][0] + sm[mi][1]), u = swap_add(sq[mi][0] + sq[mi][1]);
    float* xp = (float*)lds + K1_XP + CW * 256 + mw * 64 + mi * 32 + r;
    if (h == 0) { xp[0] = t; xp[128] = u; }
  }
  __syncthreads();
  __syncthreads();
  const float* xs = (const float*)lds + K1_XS;
  const float* cp = (const float*)lds + K1_CST + CW * 32;
  const int st = st0 + CW;
#pragma unroll
  for (int mi = 0; mi < 2; ++mi) {
    const int mt = panel * 4 + mw * 2 + mi;
    const int lr = mw * 64 + mi * 32;
    if (TYPE == 0) {
      const float mean = xs[lr + r], rstd = xs[128 + lr + r];
      const float sc = (st < 16) ? rstd * (0.25f * 1.4426950408889634f) : rstd;
      char* dstp = ws + ((st < 16) ? WS_Q : WS_K) + ((size_t)((st & 15) * 2) * 64 + mt) * 1024 + lane * 16;
#pragma unroll
      for (int hd = 0; hd < 2; ++hd) {
        const f32x4 c0 = *(const f32x4*)(cp + 16 * hd + 4 * h), c1 = *(const f32x4*)(cp + 16 * hd + 8 + 4 * h);
        float v[8];
#pragma unroll
        for (int j = 0; j < 4; ++j) { v[j] = (acc[mi][8 * hd + j] - mean * c0[j]) * sc; v[4 + j] = (acc[mi][8 * hd + 4 + j] - mean * c1[j]) * sc; }
        u32x4 o = {pk2(v[0], v[1]), pk2(v[2], v[3]), pk2(v[4], v[5]), pk2(v[6], v[7])};
        st16(dstp + (size_t)hd * 64 * 1024, o);
      }
    } else {
      const float c = cp[r];
      char* dstp = ws + WS_V + ((size_t)((st - 32) * 2 + (r >> 4)) * 64 + mt) * 1024 + h * 256 + (r & 15) * 16;
#pragma unroll
      for (int ks = 0; ks < 2; ++ks) {
        const f32x4 m0 = *(const f32x4*)(xs + lr + 16 * ks + 4 * h), m1 = *(const f32x4*)(xs + lr + 16 * ks + 8 + 4 * h);
        const f32x4 r0 = *(const f32x4*)(xs + 128 + lr + 16 * ks + 4 * h), r1 = *(const f32x4*)(xs + 128 + lr + 16 * ks + 8 + 4 * h);
        float v[8];
#pragma unroll
        for (int j = 0; j < 4; ++j) { v[j] = (acc[mi][8 * ks + j] - m0[j] * c) * r0[j]; v[4 + j] = (acc[mi][8 * ks + 4 + j] - m1[j] * c) * r1[j]; }
        u32x4 o = {pk2(v[0], v[1]), pk2(v[2], v[3]), pk2(v[4], v[5]), pk2(v[6], v[7])};
        st16(dstp + ks * 512, o);
      }
    }
  }
  ST1(18);
}

#define K1_ROT 0
__global__ __launch_bounds__(768, 3) void k1_kernel(const u16* __restrict__ x, const u16* __restrict__ wqkv, const u16* __restrict__ wo,
                                                    const u16* __restrict__ wg, const u16* __restrict__ wu, const u16* __restrict__ wd,
                                                    char* __restrict__ ws) {
  __shared__ __attribute__((aligned(16))) char lds[K1_LDS];
  const int bid = blockIdx.x;
  const int xcd = bid & 7, idx = bid >> 3;
  const int wave = __builtin_amdgcn_readfirstlane(threadIdx.x >> 6);
  const int lane = threadIdx.x & 63, c = lane & 31, h = lane >> 5;
  if (idx < 24) {
    const int panel = (xcd & 3) * 4 + (idx & 3), grp = (xcd >> 2) * 6 + (idx >> 2);
    if (wave >= 8) k1_helper(lds, panel, grp * 128, K1_ROT, x, wqkv);
    else if (grp < 8) {
      switch (wave >> 1) {
        case 0: k1_gemm<0, 0>(lds, panel, grp * 4, ws); break; case 1: k1_gemm<0, 1>(lds, panel, grp * 4, ws); break;
        case 2: k1_gemm<0, 2>(lds, panel, grp * 4, ws); break; default: k1_gemm<0, 3>(lds, panel, grp * 4, ws); break;
      }
    } else {
      switch (wave >> 1) {
        case 0: k1_gemm<1, 0>(lds, panel, grp * 4, ws); break; case 1: k1_gemm<1, 1>(lds, panel, grp * 4, ws); break;
        case 2: k1_gemm<1, 2>(lds, panel, grp * 4, ws); break; default: k1_gemm<1, 3>(lds, panel, grp * 4, ws); break;
      }
    }
  } else {
    const int pb = (idx - 24) * 8 + xcd;
    if (wave < 4) {
      u32x4 v[8];
#pragma unroll
      for (int i = 0; i < 8; ++i) v[i] = *(const u32x4*)(x + (size_t)(pb * 32 + wave * 8 + i) * D_ + lane * 8);
      float f[8][8], sm[8];
#pragma unroll
      for (int i = 0; i < 8; ++i) {
#pragma unroll
        for (int j = 0; j < 4; ++j) { f[i][2 * j] = lo16(v[i][j]); f[i][2 * j + 1] = hi16(v[i][j]); }
        sm[i] = ((f[i][0] + f[i][1]) + (f[i][2] + f[i][3])) + ((f[i][4] + f[i][5]) + (f[i][6] + f[i][7]));
      }
#pragma unroll
      for (int d = 1; d < 64; d <<= 1)
#pragma unroll
        for (int i = 0; i < 8; ++i) sm[i] += __shfl_xor(sm[i], d);
#pragma unroll
      for (int i = 0; i < 8; ++i) {
        const float mean = sm[i] * (1.f / D_);
        float q = 0.f;
#pragma unroll
        for (int j = 0; j < 8; ++j) { f[i][j] -= mean; q += f[i][j] * f[i][j]; }
        sm[i] = q;
      }
#pragma unroll
      for (int d = 1; d < 64; d <<= 1)
#pragma unroll
        for (int i = 0; i < 8; ++i) sm[i] += __shfl_xor(sm[i], d);
#pragma unroll
      for (int i = 0; i < 8; ++i) {
        const float rstd = __builtin_amdgcn_rsqf(sm[i] * (1.f / D_) + 1e-5f);
        u32x4 o;
#pragma unroll
        for (int j = 0; j < 4; ++j) o[j] = pk2(f[i][2 * j] * rstd, f[i][2 * j + 1] * rstd);
        *(u32x4*)(ws + WS_X + ((size_t)pb * 32 + (lane >> 1)) * 1024 + ((lane & 1) * 32 + wave * 8 + i) * 16) = o;
      }
    } else {
      constexpr int NG = 86 * 32, NF = NG + KST3 * 16;
      const int wv = pb * 8 + wave - 4;
      char* wl = lds + wave * 10240;
      const int kr = lane >> 2, p = lane & 3;
#pragma unroll
      for (int i = 0; i < 10; ++i) {
        int f = wv + i * 512; if (f > NF - 1) f = NF - 1;
        const u16* src;
        if (f < NG) { const int st = f >> 5, ks = f & 31; src = ((p < 2) ? wg : wu) + (size_t)(ks * 16 + kr) * F_ + st * 16 + (p & 1) * 8; }
        else { const int g = f - NG, ks = g >> 4, nt = g & 15; src = ((ks < 32) ? wo + (size_t)(ks * 16 + kr) * D_ : wd + (size_t)((ks - 32) * 16 + kr) * D_) + nt * 32 + p * 8; }
        glds16(src, wl + i * 1024);
      }
      WAITV(0);
      const lds_cptr tb = (lds_cptr)wl + ((lane & 15) >> 2) * 64 + ((lane >> 4) & 1) * 32 + (lane & 3) * 8;
#pragma unroll
      for (int i = 0; i < 10; ++i) {
        const int f = wv + i * 512;
        const bool nat = f < NG;
        const s16x4 lo = tr_read(tb + i * 1024 + (nat ? 8 * h : 4 * h) * 64), hi = tr_read(tb + i * 1024 + (nat ? 8 * h + 4 : 8 + 4 * h) * 64);
        const bf16x8 o = __builtin_shufflevector(lo, hi, 0, 1, 2, 3, 4, 5, 6, 7);
        char* d = nat ? ws + WS_G + (size_t)f * 1024 : ws + WS_W + (size_t)(f - NG) * 1024;
        if (f < NF) st16(d + lane * 16, __builtin_bit_cast(u32x4, o));
      }
    }
  }
}

constexpr int K2_V = 65536, K2_C = 131072, K2_Q = K2_C + 8192, K2_CNT = K2_Q + 8192, K2_LDS = K2_CNT + 128;
#define K2_HEAD 102.f
#define K2_INIT 8
constexpr float HEAD = K2_HEAD;
#define K2_ITERS 16

DI float cexp2(float x) { return __builtin_amdgcn_fmed3f(__builtin_amdgcn_exp2f(x), 0.f, 1.f); }
DI void exp_pack(const f32x16& s, u32x4& p0, u32x4& p1) {
#pragma unroll
  for (int i = 0; i < 4; ++i) { p0[i] = pk2(cexp2(s[2 * i]), cexp2(s[2 * i + 1])); p1[i] = pk2(cexp2(s[8 + 2 * i]), cexp2(s[9 + 2 * i])); }
}
DI float max16(const f32x16& s) {
  float m = __builtin_fmaxf(__builtin_fmaxf(s[0], s[1]), s[2]);
#pragma unroll
  for (int i = 3; i < 15; i += 2) m = __builtin_fmaxf(__builtin_fmaxf(m, s[i]), s[i + 1]);
  return __builtin_fmaxf(m, s[15]);
}
DI f32x16 splat16(float v) { f32x16 r;
#pragma unroll
  for (int i = 0; i < 16; ++i) r[i] = v;
  return r; }

DI void k2_fix(f32x16& o, float& nm, const bf16x8 qf, lds_cptr kp0, lds_cptr vp0, int vinc, int t0, int h) {
  float sm = -3.0e38f;
#pragma unroll 1
  for (int t = t0; t < t0 + 4; ++t) {
    const f32x16 s = MFMA(lds_read16(kp0 + t * 1024), qf, splat16(nm));
    sm = __builtin_fmaxf(sm, max16(s));
    u32x4 p0, p1; exp_pack(s, p0, p1);
#pragma unroll
    for (int i = 0; i < 4; ++i) { p0[i] ^= 0x80008000u; p1[i] ^= 0x80008000u; }
    o = MFMA(lds_read16(vp0 + t * vinc), BC(p0), o);
    o = MFMA(lds_read16(vp0 + t * vinc + 512), BC(p1), o);
  }
  sm = swap_max(sm);
  const float lo = swap_add(h == 0 ? o[8] : 0.f);
  sm = __builtin_fmaxf(sm, __builtin_amdgcn_logf(__builtin_fmaxf(lo, 1e-37f)));
  const float dl = __builtin_fmaxf(sm + HEAD, 0.f);
  const float f = __builtin_amdgcn_exp2f(-dl);
#pragma unroll
  for (int i = 0; i < 16; ++i) o[i] *= f;
  nm -= dl;
#pragma unroll 1
  for (int t = t0; t < t0 + 4; ++t) {
    const f32x16 s = MFMA(lds_read16(kp0 + t * 1024), qf, splat16(nm));
    u32x4 p0, p1; exp_pack(s, p0, p1);
    o = MFMA(lds_read16(vp0 + t * vinc), BC(p0), o);
    o = MFMA(lds_read16(vp0 + t * vinc + 512), BC(p1), o);
  }
}
DI void k2_sync(char* lds, int target) {
  volatile __attribute__((address_space(3))) unsigned* cnt = (volatile __attribute__((address_space(3))) unsigned*)(lds + K2_CNT);
  if ((threadIdx.x & 63) == 0) __hip_atomic_fetch_add((__attribute__((address_space(3))) unsigned*)(lds + K2_CNT), 1u, __ATOMIC_RELAXED, __HIP_MEMORY_SCOPE_WORKGROUP);
  while ((int)__builtin_amdgcn_readfirstlane(*cnt) < target) __builtin_amdgcn_s_sleep(1);
  asm volatile("" ::: "memory");
}

DI void k2_need(char* lds, int n) {
  volatile __attribute__((address_space(3))) int* pg = (volatile __attribute__((address_space(3))) int*)(lds + K2_CNT + 16);
  while (__builtin_amdgcn_readfirstlane(pg[0]) < n || __builtin_amdgcn_readfirstlane(pg[1]) < n) __builtin_amdgcn_s_sleep(1);
  asm volatile("" ::: "memory");
}
DI void k2_loader(char* lds, const char* __restrict__ ws, int which) {
  const int lane = threadIdx.x & 63, bid = blockIdx.x;
  const int head = (bid & 7) * 4 + ((bid >> 3) & 3);
  const char* g = ws + (which ? WS_V : WS_K) + (size_t)head * 65536 + lane * 16;
  char* l = lds + (which ? K2_V : 0);
  volatile __attribute__((address_space(3))) int* pg = (volatile __attribute__((address_space(3))) int*)(lds + K2_CNT + 16) + which;
  __builtin_amdgcn_s_setprio(3);
  const int c0 = which ? 0 : 1;
#pragma unroll 1
  for (int c = c0; c < 10; ++c) {
    if (c < 8) {
#pragma unroll
      for (int i = 0; i < 8; ++i) glds16(g + c * 8192 + i * 1024, l + c * 8192 + i * 1024);
    }
    if (c - 2 >= c0) {
      if (c < 8) WAITV(16); else if (c == 8) WAITV(8); else WAITV(0);
      if (lane == 0) *pg = c - 1;
    }
  }
}
#define TS(i)
DI void k2_attn(char* lds, char* __restrict__ ws) {
  const int tid = threadIdx.x, lane = tid & 63, r = lane & 31, h = lane >> 5;
  const int wave = __builtin_amdgcn_readfirstlane(tid >> 6);
  const int bid = blockIdx.x;
  const int head = (bid & 7) * 4 + ((bid >> 3) & 3), qb = bid >> 5;
  const int mt = qb * 8 + wave;
  glds16(ws + WS_Q + ((size_t)head * 64 + mt) * 1024 + lane * 16, lds + K2_Q + wave * 1024);
  glds16(ws + WS_K + (size_t)head * 65536 + wave * 1024 + lane * 16, lds + wave * 1024);
  {
    const unsigned c = ((tid & 31) == 0) ? 0x3f803f80u : 0u;
    u32x4 z = {c, c, c, c}; *(u32x4*)(lds + K2_C + tid * 16) = z;
  }
  asm volatile("s_waitcnt vmcnt(0) lgkmcnt(0)" ::: "memory");
  k2_sync(lds, 8);
  const bf16x8 qf = lds_read16((lds_cptr)lds + K2_Q + wave * 1024 + lane * 16);
  const lds_cptr kp0 = (lds_cptr)lds + lane * 16;
  const lds_cptr vp0 = (r < 16) ? (lds_cptr)lds + K2_V + h * 256 + r * 16 : (lds_cptr)lds + K2_C + (r == 16 ? 0 : 16);
  const int vinc = (r < 16) ? 1024 : 0;
  f32x16 o = f32x16{};
  float nm;
  {
    const f32x16 z = f32x16{};
    float m = max16(MFMA(lds_read16(kp0), qf, z));
#pragma unroll
    for (int t = 1; t < K2_INIT; ++t) m = __builtin_fmaxf(m, max16(MFMA(lds_read16(kp0 + t * 1024), qf, z)));
    nm = -(swap_max(m) + HEAD);
  }
  TS(0);
  k2_need(lds, 2);
  TS(1);
#define XC(x) negm
#define XU(...)
#define EXP4(S, b) do { _Pragma("unroll") for (int i = 0; i < 4; ++i) e[i] = cexp2(S[b + i]); PIN(e); SB(); } while (0)
#define EXP4P(P, S, b) do { P[0] = pk2(e[0], e[1]); P[1] = pk2(e[2], e[3]); \
    P[2] = pk2(cexp2(S[b]), cexp2(S[b + 1])); P[3] = pk2(cexp2(S[b + 2]), cexp2(S[b + 3])); PIN(P); SB(); } while (0)
#define THEAD(j, SC, SN, KN, KL, VL0, VL1) do { \
    EXP4(SC, 0); \
    SN = MFMA(KN, qf, XC(SN)); PIN(SN); SB(); \
    KL = lds_read16(kp + (j + 2) * 1024); VL0 = lds_read16(vp + (j + 1) * 1024); VL1 = lds_read16(vp + (j + 1) * 1024 + 512); SB(); \
    EXP4P(p0, SC, 4); } while (0)
#define TTAIL(SC, SN, KO, VU0, VU1) do { \
    o = MFMA(VU0, BC(p0), o); PIN(o); SB(); \
    EXP4(SC, 8); \
    XU(SN = MFMA(KO, nqf, SN); PIN(SN); SB();) \
    EXP4P(p1, SC, 12); \
    o = MFMA(VU1, BC(p1), o); PIN(o); SB(); } while (0)
  f32x4 e;
  bf16x8 nqf;
  { u32x4 t = __builtin_bit_cast(u32x4, qf); t[0] ^= 0x80008000u; t[1] ^= 0x80008000u; t[2] ^= 0x80008000u; t[3] ^= 0x80008000u; nqf = BC(t); }
  int it = 0;
#pragma unroll 1
  for (;;) {
    lds_cptr kp = kp0 + it * 4096, vp = vp0 + it * 4 * vinc;
    bf16x8 k0 = lds_read16(kp), k1 = lds_read16(kp + 1024), k2, k3 = k0;
    bf16x8 vA0 = lds_read16(vp), vA1 = lds_read16(vp + 512), vB0, vB1;
    f32x16 negm = splat16(nm); PIN(negm);
    f32x16 sA = MFMA(k0, qf, splat16(nm)), sB = sA;
    u32x4 p0, p1;
    THEAD(0, sA, sB, k1, k2, vB0, vB1);
    bool trig;
#pragma unroll 1
    do {
      TTAIL(sA, sB, k3, vA0, vA1);
      THEAD(1, sB, sA, k2, k3, vA0, vA1); TTAIL(sB, sA, k0, vB0, vB1);
      THEAD(2, sA, sB, k3, k0, vB0, vB1); TTAIL(sA, sB, k1, vA0, vA1);
      THEAD(3, sB, sA, k0, k1, vA0, vA1); TTAIL(sB, sA, k2, vB0, vB1);
      kp += 4096; vp += 4 * vinc; ++it;
      if ((it & 1) == 0 && it < 14) k2_need(lds, (it >> 1) + 2);
      {
        volatile __attribute__((address_space(3))) int* pg = (volatile __attribute__((address_space(3))) int*)(lds + K2_CNT + 64);
        if (lane == 0) pg[wave] = it;
        const int other = __builtin_amdgcn_readfirstlane(pg[wave ^ 4]);
        if (it > other) __builtin_amdgcn_s_setprio(1); else __builtin_amdgcn_s_setprio(2);
      }
      THEAD(0, sA, sB, k1, k2, vB0, vB1);
      trig = __any(o[8] >= 1.f);
    } while (it < K2_ITERS && !trig);
    if (__builtin_expect(!trig, 1)) break;
    k2_fix(o, nm, qf, kp0, vp0, vinc, 4 * it - 4, h);
    if (it >= K2_ITERS) break;
  }
  asm volatile("s_nop 15" : "+v"(o));
  const float il = 1.f / swap_add(h == 0 ? o[8] : 0.f);
  u32x4 ov = {pk2(mul_s(o[0], il), mul_s(o[1], il)), pk2(mul_s(o[2], il), mul_s(o[3], il)), pk2(mul_s(o[4], il), mul_s(o[5], il)), pk2(mul_s(o[6], il), mul_s(o[7], il))};
  st16(ws + WS_A + ((size_t)mt * KST3 + head) * 1024 + lane * 16, ov);
}

DI void k2_ffn(char* __restrict__ ws) {
  const int lane = threadIdx.x & 63;
  const int fw = __builtin_amdgcn_readfirstlane(threadIdx.x >> 6) - 8;
  const int bid = blockIdx.x;
  const int sp = (bid & 7) + 8 * fw, mp = bid >> 3;
  if (sp < 43) {
    const __amdgpu_buffer_rsrc_t rs = __builtin_amdgcn_make_buffer_rsrc(ws, 0, 0x7fffffff, 0x00020000);
    const int xo = (int)WS_X + mp * 65536, wo = (int)WS_G + sp * 65536, l16 = lane * 16;
    f32x16 acc[2][2] = {{f32x16{}, f32x16{}}, {f32x16{}, f32x16{}}};
#define FF_PF 3
    constexpr int PF = FF_PF;
    u32x4 a0[PF], a1[PF], b0[PF], b1[PF];
#define BLD(off) __builtin_amdgcn_raw_buffer_load_b128(rs, l16, (off), 0)
#define FF_LOAD(i, k) do { a0[i] = BLD(xo + (k) * 1024); a1[i] = BLD(xo + 32768 + (k) * 1024); \
    b0[i] = BLD(wo + (k) * 1024); b1[i] = BLD(wo + 32768 + (k) * 1024); } while (0)
#pragma unroll
    for (int i = 0; i < PF; ++i) FF_LOAD(i, i);
#pragma unroll
    for (int k = 0; k < 32; ++k) {
      const int i = k % PF;
      acc[0][0] = MFMA(BC(b0[i]), BC(a0[i]), acc[0][0]); acc[0][1] = MFMA(BC(b1[i]), BC(a0[i]), acc[0][1]);
      acc[1][0] = MFMA(BC(b0[i]), BC(a1[i]), acc[1][0]); acc[1][1] = MFMA(BC(b1[i]), BC(a1[i]), acc[1][1]);
      PIN(acc[1][1]); SB();
      if (k + PF < 32) FF_LOAD(i, k + PF);
      SB();
    }
    asm volatile("s_nop 15\n\ts_nop 15" : "+v"(acc[0][0]), "+v"(acc[0][1]), "+v"(acc[1][0]), "+v"(acc[1][1]));
    SB();
#pragma unroll
    for (int mi = 0; mi < 2; ++mi)
#pragma unroll
      for (int s = 0; s < 2; ++s) {
        float v[8];
#pragma unroll
        for (int j = 0; j < 8; ++j) {
          const float g = acc[mi][s][j], u = acc[mi][s][8 + j];
          float d = __builtin_amdgcn_exp2f(mul_s(g, -1.4426950408889634f)); add_s(d, 1.f);
          v[j] = mul_s(mul_s(g, __builtin_amdgcn_rcpf(d)), u);
        }
        u32x4 o = {pk2(v[0], v[1]), pk2(v[2], v[3]), pk2(v[4], v[5]), pk2(v[6], v[7])};
        st16(ws + WS_A + ((size_t)(mp * 2 + mi) * KST3 + 32 + sp * 2 + s) * 1024 + lane * 16, o);
      }
  }
}

__global__ __launch_bounds__(1024, 4) void k2_kernel(char* __restrict__ ws) {
  __shared__ __attribute__((aligned(16))) char lds[K2_LDS];
  if (threadIdx.x < 32) *(unsigned*)(lds + K2_CNT + threadIdx.x * 4) = 0u;
  __syncthreads();
  const int wv = __builtin_amdgcn_readfirstlane(threadIdx.x >> 6);
  if (wv < 8) k2_attn(lds, ws); else if (wv < 14) k2_ffn(ws); else k2_loader(lds, ws, wv - 14);
}

constexpr int K3_ROWB = 64 * 4 + 16;
#define K3_PF 6
#define K3_ROT 0
__global__ __launch_bounds__(256, 1) void k3_kernel(const u16* __restrict__ x, char* __restrict__ ws, u16* __restrict__ out) {
  __shared__ __attribute__((aligned(16))) char lds[4 * 64 * K3_ROWB];
  const int tid = threadIdx.x, lane = tid & 63, r = lane & 31, h = lane >> 5;
  const int wave = __builtin_amdgcn_readfirstlane(tid >> 6);
  const int bid = blockIdx.x;
  const int mb = (bid & 7) * 4 + ((bid >> 3) & 3), nb = bid >> 5;
  const int kq = (wave + (K3_ROT)) & 3;
  const int k0 = kq * 30 - (kq > 2 ? 1 : 0), nk = (kq < 2) ? 30 : 29;
  const __amdgpu_buffer_rsrc_t rs = __builtin_amdgcn_make_buffer_rsrc(ws, 0, 0x7fffffff, 0x00020000);
  const int ao = (int)WS_A + (mb * 2 * KST3 + k0) * 1024, wo = (int)WS_W + (k0 * 16 + nb * 2) * 1024, l16 = lane * 16;
  const int m = tid >> 2, n0 = (tid & 3) * 16;
  const size_t go = (size_t)(mb * 64 + m) * D_ + nb * 64 + n0;
  f32x16 acc[2][2] = {{f32x16{}, f32x16{}}, {f32x16{}, f32x16{}}};
  constexpr int PF = K3_PF;
  u32x4 a0[PF], a1[PF], b0[PF], b1[PF];
  const int klast = nk - 1;
#define K3_LOAD(i, k) do { const int kk_ = ((k) < 29) ? (k) : klast; \
    a0[i] = BLD(ao + kk_ * 1024); a1[i] = BLD(ao + KST3 * 1024 + kk_ * 1024); \
    b0[i] = BLD(wo + kk_ * 16384); b1[i] = BLD(wo + kk_ * 16384 + 1024); } while (0)
#pragma unroll
  for (int i = 0; i < PF; ++i) K3_LOAD(i, i);
#pragma unroll
  for (int k = 0; k < 30; ++k) {
    const int i = k % PF;
    if (k < 29 || nk == 30) {
      acc[0][0] = MFMA(BC(b0[i]), BC(a0[i]), acc[0][0]); acc[0][1] = MFMA(BC(b1[i]), BC(a0[i]), acc[0][1]);
      acc[1][0] = MFMA(BC(b0[i]), BC(a1[i]), acc[1][0]); acc[1][1] = MFMA(BC(b1[i]), BC(a1[i]), acc[1][1]);
    }
    if (k + PF < 30) K3_LOAD(i, k + PF);
    SB();
  }
  const u32x4 x0 = *(const u32x4*)(x + go), x1 = *(const u32x4*)(x + go + 8);
  char* pw = lds + wave * 64 * K3_ROWB;
#pragma unroll
  for (int i = 0; i < 2; ++i)
#pragma unroll
    for (int j = 0; j < 2; ++j)
#pragma unroll
      for (int g = 0; g < 4; ++g) {
        f32x4 v = {acc[i][j][4 * g], acc[i][j][4 * g + 1], acc[i][j][4 * g + 2], acc[i][j][4 * g + 3]};
        *(f32x4*)(pw + (i * 32 + r) * K3_ROWB + (j * 32 + 8 * g + 4 * h) * 4) = v;
      }
  __syncthreads();
  float s[16];
#pragma unroll
  for (int j = 0; j < 4; ++j) {
    s[2 * j] = lo16(x0[j]); s[2 * j + 1] = hi16(x0[j]);
    s[8 + 2 * j] = lo16(x1[j]); s[8 + 2 * j + 1] = hi16(x1[j]);
  }
#pragma unroll
  for (int w = 0; w < 4; ++w)
#pragma unroll
    for (int q = 0; q < 4; ++q) {
      const f32x4 v = *(const f32x4*)(lds + w * 64 * K3_ROWB + m * K3_ROWB + (n0 + 4 * q) * 4);
#pragma unroll
      for (int j = 0; j < 4; ++j) s[4 * q + j] += v[j];
    }
  u32x4 o0 = {pk2(s[0], s[1]), pk2(s[2], s[3]), pk2(s[4], s[5]), pk2(s[6], s[7])};
  u32x4 o1 = {pk2(s[8], s[9]), pk2(s[10], s[11]), pk2(s[12], s[13]), pk2(s[14], s[15])};
  *(u32x4*)(out + go) = o0; *(u32x4*)(out + go + 8) = o1;
}

extern "C" void kernel_launch(void* const* d_in, const int* in_sizes, int n_in, void* d_out, int out_size, void* d_ws, size_t ws_size,
                              hipStream_t stream) {
  const u16* x = (const u16*)d_in[0];
  const u16* wqkv = (const u16*)d_in[1];
  const u16* wo = (const u16*)d_in[2];
  const u16* wg = (const u16*)d_in[4];
  const u16* wu = (const u16*)d_in[5];
  const u16* wd = (const u16*)d_in[6];
  char* ws = (char*)d_ws;
  k1_kernel<<<256, 768, 0, stream>>>(x, wqkv, wo, wg, wu, wd, ws);
  k2_kernel<<<256, 1024, 0, stream>>>(ws);
  k3_kernel<<<256, 256, 0, stream>>>(x, ws, (u16*)d_out);
}
```

```cpp
#include <hip/hip_runtime.h>
#include <hip/hip_bf16.h>

typedef short bf16x8 __attribute__((ext_vector_type(8)));
typedef short s16x4 __attribute__((ext_vector_type(4)));
typedef float f32x16 __attribute__((ext_vector_type(16)));
typedef float f32x4 __attribute__((ext_vector_type(4)));
typedef unsigned u32x4 __attribute__((ext_vector_type(4)));
typedef unsigned u32x2 __attribute__((ext_vector_type(2)));
typedef unsigned short u16;
typedef __attribute__((address_space(3))) const char* lds_cptr;
typedef __bf16 bf16x2_t __attribute__((ext_vector_type(2)));
typedef float f32x2_t __attribute__((ext_vector_type(2)));

#define DI __device__ __forceinline__
#define MFMA(a, b, c) __builtin_amdgcn_mfma_f32_32x32x16_bf16((a), (b), (c), 0, 0, 0)
#define BC(x) __builtin_bit_cast(bf16x8, x)
#define WAITV(n) asm volatile("s_waitcnt vmcnt(" #n ")" ::: "memory")
#define BAR() __builtin_amdgcn_s_barrier()
#define SB() __builtin_amdgcn_sched_barrier(0)
#define PIN(x) asm volatile("" : "+v"(x))

constexpr int S_ = 2048, D_ = 512, F_ = 1376, NH = 32;
constexpr int KST3 = 118;
constexpr size_t WS_Q = 0;
constexpr size_t WS_K = WS_Q + (size_t)NH * 64 * 1024;
constexpr size_t WS_V = WS_K + (size_t)NH * 64 * 1024;
constexpr size_t WS_A = WS_V + (size_t)NH * 64 * 1024;
constexpr size_t WS_W = WS_A + (size_t)64 * KST3 * 1024;
constexpr size_t WS_X = WS_W + (size_t)KST3 * 16 * 1024;
constexpr size_t WS_G = WS_X + (size_t)64 * 32 * 1024;

DI unsigned pk2(float a, float b) { f32x2_t v = {a, b}; return __builtin_bit_cast(unsigned, __builtin_convertvector(v, bf16x2_t)); }
DI float swap_add(float v) {
  auto rr = __builtin_amdgcn_permlane32_swap(__float_as_uint(v), __float_as_uint(v), false, false);
  return __uint_as_float(rr[0]) + __uint_as_float(rr[1]);
}
DI float swap_max(float v) {
  auto rr = __builtin_amdgcn_permlane32_swap(__float_as_uint(v), __float_as_uint(v), false, false);
  return __builtin_fmaxf(__uint_as_float(rr[0]), __uint_as_float(rr[1]));
}
DI s16x4 tr_read(lds_cptr p) {
  typedef short v4i16_t __attribute__((ext_vector_type(4)));
  return __builtin_bit_cast(s16x4, __builtin_amdgcn_ds_read_tr16_b64_v4i16((__attribute__((address_space(3))) v4i16_t*)p));
}
DI bf16x8 lds_read16(lds_cptr p) { return *(const __attribute__((address_space(3))) bf16x8*)p; }
DI void glds16(const void* g, char* lds) {
  __builtin_amdgcn_global_load_lds((const __attribute__((address_space(1))) void*)g, (__attribute__((address_space(3))) void*)lds, 16, 0, 0);
}
DI void add_s(float& a, float b) { a = a + b; asm("" : "+v"(a)); }
DI void fmac_s(float& a, float b, float c) { a = __builtin_fmaf(b, c, a); asm("" : "+v"(a)); }
DI float mul_s(float a, float b) { float r = a * b; asm("" : "+v"(r)); return r; }
#define ST_MODE 0
DI void st16(void* p, u32x4 v) {
  *(u32x4*)p = v;
}
DI float lo16(unsigned u) { return __uint_as_float(u << 16); }
DI float hi16(unsigned u) { return __uint_as_float(u & 0xffff0000u); }

#define ST1(i)

constexpr int K1_AB = 16384;
constexpr int K1_SLOT = 32768;
constexpr int K1_NSLOT = 4;
constexpr int K1_LDS = K1_NSLOT * K1_SLOT;
constexpr int K1_XS = 0;
constexpr int K1_CSP = 256;
constexpr int K1_CST = K1_CSP + 16 * 128;
constexpr int K1_XP = K1_CST + 128;

DI void k1_helper(char* lds, int panel, int col0, int rot, const u16* __restrict__ x, const u16* __restrict__ w) {
  const int lane = threadIdx.x & 63;
  const int hw = __builtin_amdgcn_readfirstlane(threadIdx.x >> 6) - 8;
  const u16 *sa, *sb;
  {
    const int row = hw * 32 + (lane >> 3), ch = (lane & 7) ^ ((row >> 1) & 3);
    sa = x + (size_t)(panel * 128 + row) * D_ + ch * 8;
    const int kr = hw * 16 + (lane >> 4), pc = (lane & 15) ^ ((kr & 3) << 2);
    sb = w + (size_t)kr * (3 * D_) + col0 + pc * 8;
  }
  const int a4 = ((lane & 7) ^ (((lane >> 4) & 3) | 4)) * 8 - ((lane & 7) ^ ((lane >> 4) & 3)) * 8;
  auto stage = [&](int kc) {
    char* d = lds + (kc % K1_NSLOT) * K1_SLOT + hw * 4096;
    const int k0 = ((kc + rot) & 7) * 64;
#pragma unroll
    for (int i = 0; i < 4; ++i) glds16(sa + k0 + i * 8 * D_ + ((i & 1) ? a4 : 0), d + i * 1024);
#pragma unroll
    for (int i = 0; i < 4; ++i) glds16(sb + (size_t)(k0 + i * 4) * (3 * D_), d + K1_AB + i * 1024);
  };
  const int g = lane >> 4, pc = lane & 15;
  const lds_cptr cr = (lds_cptr)lds + K1_AB + (16 * hw + g) * 256 + ((pc ^ (g << 2)) << 4);
  float cs[8];
#pragma unroll
  for (int c = 0; c < 8; ++c) cs[c] = 0.f;
  stage(0); stage(1);
  WAITV(8);
  BAR();
  stage(2);
#pragma unroll 1
  for (int kc = 0; kc < 8; ++kc) {
    if (kc < 6) WAITV(8); else WAITV(0);
    BAR();
    if (kc + 3 < 8) stage(kc + 3);
    u32x4 b[4];
#pragma unroll
    for (int i = 0; i < 4; ++i) b[i] = __builtin_bit_cast(u32x4, lds_read16(cr + (kc % K1_NSLOT) * K1_SLOT + i * 1024));
#pragma unroll
    for (int i = 0; i < 4; ++i)
#pragma unroll
      for (int j = 0; j < 4; ++j) { add_s(cs[2 * j], lo16(b[i][j])); add_s(cs[2 * j + 1], hi16(b[i][j])); }
  }
  float* sc = (float*)lds;
  {
    f32x4 c0 = {cs[0], cs[1], cs[2], cs[3]}, c1 = {cs[4], cs[5], cs[6], cs[7]};
    float* d = sc + K1_CSP + (4 * hw + g) * 128 + pc * 8;
    *(f32x4*)d = c0; *(f32x4*)(d + 4) = c1;
  }
  __syncthreads();
  if (hw < 2) {
    const int i = hw * 64 + lane;
    float t = 0.f;
#pragma unroll
    for (int q = 0; q < 16; ++q) t += sc[K1_CSP + q * 128 + i];
    sc[K1_CST + i] = t;
  } else {
    const int i = (hw - 2) * 64 + lane;
    const float* p = sc + K1_XP + i;
    const float mean = ((p[0] + p[256]) + (p[512] + p[768])) * (1.f / D_);
    const float var = __builtin_fmaxf(((p[128] + p[384]) + (p[640] + p[896])) * (1.f / D_) - mean * mean, 0.f);
    sc[K1_XS + i] = mean; sc[K1_XS + 128 + i] = __builtin_amdgcn_rsqf(var + 1e-5f);
  }
  __syncthreads();
}

template <int TYPE, int CW>
DI void k1_gemm(char* lds, int panel, int st0, char* __restrict__ ws) {
  const int tid = threadIdx.x, lane = tid & 63, r = lane & 31, h = lane >> 5;
  const int mw = __builtin_amdgcn_readfirstlane(tid >> 6) & 1;
  f32x16 acc[2] = {f32x16{}, f32x16{}};
  const int arow = mw * 64 + r;
  const int aswz = (arow >> 1) & 7;
  lds_cptr ab[4];
#pragma unroll
  for (int ks = 0; ks < 4; ++ks) ab[ks] = (lds_cptr)lds + arow * 128 + (((2 * ks + h) ^ aswz) << 4);
  const lds_cptr bb = (lds_cptr)lds + K1_AB + (8 * h + ((lane & 15) >> 2)) * 256 + (CW ^ ((lane & 15) >> 2)) * 64 + ((lane >> 4) & 1) * 32 + (lane & 3) * 8;
  bf16x8 af[2][4][2]; s16x4 bl[2][4], bh[2][4];
  float sm[2][2] = {{0.f, 0.f}, {0.f, 0.f}}, sq[2][2] = {{0.f, 0.f}, {0.f, 0.f}};
#define K1_FRAGS(buf, soff) do { __builtin_amdgcn_s_waitcnt(0xC07F); \
    _Pragma("unroll") for (int ks = 0; ks < 4; ++ks) { const lds_cptr a_ = ab[ks] + (soff); \
      af[buf][ks][0] = lds_read16(a_); af[buf][ks][1] = lds_read16(a_ + 4096); \
      bl[buf][ks] = tr_read(bb + (soff) + ks * 4096); bh[buf][ks] = tr_read(bb + (soff) + ks * 4096 + 1024); } SB(); } while (0)
#define K1_MMA(buf) do { _Pragma("unroll") for (int ks = 0; ks < 4; ++ks) { \
    const bf16x8 bf = __builtin_shufflevector(bl[buf][ks], bh[buf][ks], 0, 1, 2, 3, 4, 5, 6, 7); \
    _Pragma("unroll") for (int mi = 0; mi < 2; ++mi) { \
      if (TYPE == 1) acc[mi] = MFMA(af[buf][ks][mi], bf, acc[mi]); else acc[mi] = MFMA(bf, af[buf][ks][mi], acc[mi]); \
      const unsigned u = __builtin_bit_cast(u32x4, af[buf][ks][mi])[CW]; const float lo = lo16(u), hi = hi16(u); \
      add_s(sm[mi][0], lo); add_s(sm[mi][1], hi); fmac_s(sq[mi][0], lo, lo); fmac_s(sq[mi][1], hi, hi); \
      asm volatile("" : "+v"(sm[mi][0]), "+v"(sm[mi][1]), "+v"(sq[mi][0]), "+v"(sq[mi][1])); SB(); } } } while (0)
  ST1(0);
  BAR();
  ST1(1);
  K1_FRAGS(0, 0);
#pragma unroll 1
  for (int kc = 0; kc < 8; kc += 2) {
    BAR();
    K1_FRAGS(1, ((kc + 1) & 3) * K1_SLOT); K1_MMA(0);
    BAR();
    K1_FRAGS(0, ((kc + 2) & 3) * K1_SLOT); K1_MMA(1);
  }
  ST1(17);
#pragma unroll
  for (int mi = 0; mi < 2; ++mi) {
    const float t = swap_add(sm[mi][0] + sm[mi][1]), u = swap_add(sq[mi][0] + sq[mi][1]);
    float* xp = (float*)lds + K1_XP + CW * 256 + mw * 64 + mi * 32 + r;
    if (h == 0) { xp[0] = t; xp[128] = u; }
  }
  __syncthreads();
  __syncthreads();
  const float* xs = (const float*)lds + K1_XS;
  const float* cp = (const float*)lds + K1_CST + CW * 32;
  const int st = st0 + CW;
#pragma unroll
  for (int mi = 0; mi < 2; ++mi) {
    const int mt = panel * 4 + mw * 2 + mi;
    const int lr = mw * 64 + mi * 32;
    if (TYPE == 0) {
      const float mean = xs[lr + r], rstd = xs[128 + lr + r];
      const float sc = (st < 16) ? rstd * (0.25f * 1.4426950408889634f) : rstd;
      char* dstp = ws + ((st < 16) ? WS_Q : WS_K) + ((size_t)((st & 15) * 2) * 64 + mt) * 1024 + lane * 16;
#pragma unroll
      for (int hd = 0; hd < 2; ++hd) {
        const f32x4 c0 = *(const f32x4*)(cp + 16 * hd + 4 * h), c1 = *(const f32x4*)(cp + 16 * hd + 8 + 4 * h);
        float v[8];
#pragma unroll
        for (int j = 0; j < 4; ++j) { v[j] = (acc[mi][8 * hd + j] - mean * c0[j]) * sc; v[4 + j] = (acc[mi][8 * hd + 4 + j] - mean * c1[j]) * sc; }
        u32x4 o = {pk2(v[0], v[1]), pk2(v[2], v[3]), pk2(v[4], v[5]), pk2(v[6], v[7])};
        st16(dstp + (size_t)hd * 64 * 1024, o);
      }
    } else {
      const float c = cp[r];
      char* dstp = ws + WS_V + ((size_t)((st - 32) * 2 + (r >> 4)) * 64 + mt) * 1024 + h * 256 + (r & 15) * 16;
#pragma unroll
      for (int ks = 0; ks < 2; ++ks) {
        const f32x4 m0 = *(const f32x4*)(xs + lr + 16 * ks + 4 * h), m1 = *(const f32x4*)(xs + lr + 16 * ks + 8 + 4 * h);
        const f32x4 r0 = *(const f32x4*)(xs + 128 + lr + 16 * ks + 4 * h), r1 = *(const f32x4*)(xs + 128 + lr + 16 * ks + 8 + 4 * h);
        float v[8];
#pragma unroll
        for (int j = 0; j < 4; ++j) { v[j] = (acc[mi][8 * ks + j] - m0[j] * c) * r0[j]; v[4 + j] = (acc[mi][8 * ks + 4 + j] - m1[j] * c) * r1[j]; }
        u32x4 o = {pk2(v[0], v[1]), pk2(v[2], v[3]), pk2(v[4], v[5]), pk2(v[6], v[7])};
        st16(dstp + ks * 512, o);
      }
    }
  }
  ST1(18);
}

#define K1_ROT 0
__global__ __launch_bounds__(768, 3) void k1_kernel(const u16* __restrict__ x, const u16* __restrict__ wqkv,
                                                    const u16* __restrict__ wg, const u16* __restrict__ wu, char* __restrict__ ws) {
  __shared__ __attribute__((aligned(16))) char lds[K1_LDS];
  const int bid = blockIdx.x;
  const int xcd = bid & 7, idx = bid >> 3;
  const int wave = __builtin_amdgcn_readfirstlane(threadIdx.x >> 6);
  const int lane = threadIdx.x & 63, c = lane & 31, h = lane >> 5;
  if (idx < 24) {
    const int panel = (xcd & 3) * 4 + (idx & 3), grp = (xcd >> 2) * 6 + (idx >> 2);
    if (wave >= 8) k1_helper(lds, panel, grp * 128, K1_ROT, x, wqkv);
    else if (grp < 8) {
      switch (wave >> 1) {
        case 0: k1_gemm<0, 0>(lds, panel, grp * 4, ws); break; case 1: k1_gemm<0, 1>(lds, panel, grp * 4, ws); break;
        case 2: k1_gemm<0, 2>(lds, panel, grp * 4, ws); break; default: k1_gemm<0, 3>(lds, panel, grp * 4, ws); break;
      }
    } else {
      switch (wave >> 1) {
        case 0: k1_gemm<1, 0>(lds, panel, grp * 4, ws); break; case 1: k1_gemm<1, 1>(lds, panel, grp * 4, ws); break;
        case 2: k1_gemm<1, 2>(lds, panel, grp * 4, ws); break; default: k1_gemm<1, 3>(lds, panel, grp * 4, ws); break;
      }
    }
  } else {
    const int pb = (idx - 24) * 8 + xcd;
    if (wave < 4) {
      u32x4 v[8];
#pragma unroll
      for (int i = 0; i < 8; ++i) v[i] = *(const u32x4*)(x + (size_t)(pb * 32 + wave * 8 + i) * D_ + lane * 8);
      float f[8][8], sm[8];
#pragma unroll
      for (int i = 0; i < 8; ++i) {
#pragma unroll
        for (int j = 0; j < 4; ++j) { f[i][2 * j] = lo16(v[i][j]); f[i][2 * j + 1] = hi16(v[i][j]); }
        sm[i] = ((f[i][0] + f[i][1]) + (f[i][2] + f[i][3])) + ((f[i][4] + f[i][5]) + (f[i][6] + f[i][7]));
      }
#pragma unroll
      for (int d = 1; d < 64; d <<= 1)
#pragma unroll
        for (int i = 0; i < 8; ++i) sm[i] += __shfl_xor(sm[i], d);
#pragma unroll
      for (int i = 0; i < 8; ++i) {
        const float mean = sm[i] * (1.f / D_);
        float q = 0.f;
#pragma unroll
        for (int j = 0; j < 8; ++j) { f[i][j] -= mean; q += f[i][j] * f[i][j]; }
        sm[i] = q;
      }
#pragma unroll
      for (int d = 1; d < 64; d <<= 1)
#pragma unroll
        for (int i = 0; i < 8; ++i) sm[i] += __shfl_xor(sm[i], d);
#pragma unroll
      for (int i = 0; i < 8; ++i) {
        const float rstd = __builtin_amdgcn_rsqf(sm[i] * (1.f / D_) + 1e-5f);
        u32x4 o;
#pragma unroll
        for (int j = 0; j < 4; ++j) o[j] = pk2(f[i][2 * j] * rstd, f[i][2 * j + 1] * rstd);
        *(u32x4*)(ws + WS_X + ((size_t)pb * 32 + (lane >> 1)) * 1024 + ((lane & 1) * 32 + wave * 8 + i) * 16) = o;
      }
    } else {
      constexpr int NG = 86 * 32;
      const int wv = pb * 8 + wave - 4;
      char* wl = lds + wave * 8192;
      const int kr = lane >> 2, p = lane & 3;
#pragma unroll
      for (int i = 0; i < 6; ++i) {
        int f = wv + i * 512; if (f > NG - 1) f = NG - 1;
        const int st = f >> 5, ks = f & 31;
        glds16(((p < 2) ? wg : wu) + (size_t)(ks * 16 + kr) * F_ + st * 16 + (p & 1) * 8, wl + i * 1024);
      }
      WAITV(0);
      const lds_cptr tb = (lds_cptr)wl + (8 * h + ((lane & 15) >> 2)) * 64 + ((lane >> 4) & 1) * 32 + (lane & 3) * 8;
#pragma unroll
      for (int i = 0; i < 6; ++i) {
        const int f = wv + i * 512;
        const bf16x8 o = __builtin_shufflevector(tr_read(tb + i * 1024), tr_read(tb + i * 1024 + 256), 0, 1, 2, 3, 4, 5, 6, 7);
        if (f < NG) st16(ws + WS_G + (size_t)f * 1024 + lane * 16, __builtin_bit_cast(u32x4, o));
      }
    }
  }
}

constexpr int K2_V = 65536, K2_C = 131072, K2_Q = K2_C + 8192, K2_T = K2_Q + 8192, K2_CNT = K2_T + 8192, K2_LDS = K2_CNT + 128;
#define K2_HEAD 102.f
#define K2_INIT 8
constexpr float HEAD = K2_HEAD;
#define K2_ITERS 16

DI float cexp2(float x) { return __builtin_amdgcn_fmed3f(__builtin_amdgcn_exp2f(x), 0.f, 1.f); }
DI void exp_pack(const f32x16& s, u32x4& p0, u32x4& p1) {
#pragma unroll
  for (int i = 0; i < 4; ++i) { p0[i] = pk2(cexp2(s[2 * i]), cexp2(s[2 * i + 1])); p1[i] = pk2(cexp2(s[8 + 2 * i]), cexp2(s[9 + 2 * i])); }
}
DI float max16(const f32x16& s) {
  float m = __builtin_fmaxf(__builtin_fmaxf(s[0], s[1]), s[2]);
#pragma unroll
  for (int i = 3; i < 15; i += 2) m = __builtin_fmaxf(__builtin_fmaxf(m, s[i]), s[i + 1]);
  return __builtin_fmaxf(m, s[15]);
}
DI f32x16 splat16(float v) { f32x16 r;
#pragma unroll
  for (int i = 0; i < 16; ++i) r[i] = v;
  return r; }

DI void k2_fix(f32x16& o, float& nm, const bf16x8 qf, lds_cptr kp0, lds_cptr vp0, int vinc, int t0, int h) {
  float sm = -3.0e38f;
#pragma unroll 1
  for (int t = t0; t < t0 + 4; ++t) {
    const f32x16 s = MFMA(lds_read16(kp0 + t * 1024), qf, splat16(nm));
    sm = __builtin_fmaxf(sm, max16(s));
    u32x4 p0, p1; exp_pack(s, p0, p1);
#pragma unroll
    for (int i = 0; i < 4; ++i) { p0[i] ^= 0x80008000u; p1[i] ^= 0x80008000u; }
    o = MFMA(lds_read16(vp0 + t * vinc), BC(p0), o);
    o = MFMA(lds_read16(vp0 + t * vinc + 512), BC(p1), o);
  }
  sm = swap_max(sm);
  const float lo = swap_add(h == 0 ? o[8] : 0.f);
  sm = __builtin_fmaxf(sm, __builtin_amdgcn_logf(__builtin_fmaxf(lo, 1e-37f)));
  const float dl = __builtin_fmaxf(sm + HEAD, 0.f);
  const float f = __builtin_amdgcn_exp2f(-dl);
#pragma unroll
  for (int i = 0; i < 16; ++i) o[i] *= f;
  nm -= dl;
#pragma unroll 1
  for (int t = t0; t < t0 + 4; ++t) {
    const f32x16 s = MFMA(lds_read16(kp0 + t * 1024), qf, splat16(nm));
    u32x4 p0, p1; exp_pack(s, p0, p1);
    o = MFMA(lds_read16(vp0 + t * vinc), BC(p0), o);
    o = MFMA(lds_read16(vp0 + t * vinc + 512), BC(p1), o);
  }
}
DI void k2_sync(char* lds, int target) {
  volatile __attribute__((address_space(3))) unsigned* cnt = (volatile __attribute__((address_space(3))) unsigned*)(lds + K2_CNT);
  if ((threadIdx.x & 63) == 0) __hip_atomic_fetch_add((__attribute__((address_space(3))) unsigned*)(lds + K2_CNT), 1u, __ATOMIC_RELAXED, __HIP_MEMORY_SCOPE_WORKGROUP);
  while ((int)__builtin_amdgcn_readfirstlane(*cnt) < target) __builtin_amdgcn_s_sleep(1);
  asm volatile("" ::: "memory");
}

DI void k2_need(char* lds, int n) {
  volatile __attribute__((address_space(3))) int* pg = (volatile __attribute__((address_space(3))) int*)(lds + K2_CNT + 16);
  while (__builtin_amdgcn_readfirstlane(pg[0]) < n || __builtin_amdgcn_readfirstlane(pg[1]) < n) __builtin_amdgcn_s_sleep(1);
  asm volatile("" ::: "memory");
}
DI void k2_loader(char* lds, char* __restrict__ ws, int which, const u16* __restrict__ wo, const u16* __restrict__ wd) {
  const int lane = threadIdx.x & 63, bid = blockIdx.x;
  const int head = (bid & 7) * 4 + ((bid >> 3) & 3);
  const char* g = ws + (which ? WS_V : WS_K) + (size_t)head * 65536 + lane * 16;
  char* l = lds + (which ? K2_V : 0);
  volatile __attribute__((address_space(3))) int* pg = (volatile __attribute__((address_space(3))) int*)(lds + K2_CNT + 16) + which;
  __builtin_amdgcn_s_setprio(3);
  const int c0 = which ? 0 : 1;
#pragma unroll 1
  for (int c = c0; c < 10; ++c) {
    if (c < 8) {
#pragma unroll
      for (int i = 0; i < 8; ++i) glds16(g + c * 8192 + i * 1024, l + c * 8192 + i * 1024);
    }
    if (c - 2 >= c0) {
      if (c < 8) WAITV(16); else if (c == 8) WAITV(8); else WAITV(0);
      if (lane == 0) *pg = c - 1;
    }
  }
  __builtin_amdgcn_s_setprio(0);
  constexpr int NF = KST3 * 16;
  const int wv = bid * 2 + which, kr = lane >> 2, p = lane & 3, h = lane >> 5;
  char* wl = lds + K2_T + which * 4096;
#pragma unroll
  for (int i = 0; i < 4; ++i) {
    int f = wv + i * 512; if (f > NF - 1) f = NF - 1;
    const int ks = f >> 4, nt = f & 15;
    glds16(((ks < 32) ? wo + (size_t)(ks * 16 + kr) * D_ : wd + (size_t)((ks - 32) * 16 + kr) * D_) + nt * 32 + p * 8, wl + i * 1024);
  }
  WAITV(0);
  const lds_cptr tb = (lds_cptr)wl + (4 * h + ((lane & 15) >> 2)) * 64 + ((lane >> 4) & 1) * 32 + (lane & 3) * 8;
#pragma unroll
  for (int i = 0; i < 4; ++i) {
    const int f = wv + i * 512;
    const bf16x8 o = __builtin_shufflevector(tr_read(tb + i * 1024), tr_read(tb + i * 1024 + 512), 0, 1, 2, 3, 4, 5, 6, 7);
    if (f < NF) st16(ws + WS_W + (size_t)f * 1024 + lane * 16, __builtin_bit_cast(u32x4, o));
  }
}
#define TS(i)
DI void k2_attn(char* lds, char* __restrict__ ws) {
  const int tid = threadIdx.x, lane = tid & 63, r = lane & 31, h = lane >> 5;
  const int wave = __builtin_amdgcn_readfirstlane(tid >> 6);
  const int bid = blockIdx.x;
  const int head = (bid & 7) * 4 + ((bid >> 3) & 3), qb = bid >> 5;
  const int mt = qb * 8 + wave;
  glds16(ws + WS_Q + ((size_t)head * 64 + mt) * 1024 + lane * 16, lds + K2_Q + wave * 1024);
  glds16(ws + WS_K + (size_t)head * 65536 + wave * 1024 + lane * 16, lds + wave * 1024);
  {
    const unsigned c = ((tid & 31) == 0) ? 0x3f803f80u : 0u;
    u32x4 z = {c, c, c, c}; *(u32x4*)(lds + K2_C + tid * 16) = z;
  }
  asm volatile("s_waitcnt vmcnt(0) lgkmcnt(0)" ::: "memory");
  k2_sync(lds, 8);
  const bf16x8 qf = lds_read16((lds_cptr)lds + K2_Q + wave * 1024 + lane * 16);
  const lds_cptr kp0 = (lds_cptr)lds + lane * 16;
  const lds_cptr vp0 = (r < 16) ? (lds_cptr)lds + K2_V + h * 256 + r * 16 : (lds_cptr)lds + K2_C + (r == 16 ? 0 : 16);
  const int vinc = (r < 16) ? 1024 : 0;
  f32x16 o = f32x16{};
  float nm;
  {
    const f32x16 z = f32x16{};
    float m = max16(MFMA(lds_read16(kp0), qf, z));
#pragma unroll
    for (int t = 1; t < K2_INIT; ++t) m = __builtin_fmaxf(m, max16(MFMA(lds_read16(kp0 + t * 1024), qf, z)));
    nm = -(swap_max(m) + HEAD);
  }
  TS(0);
  k2_need(lds, 2);
  TS(1);
#define XC(x) negm
#define XU(...)
#define EXP4(S, b) do { _Pragma("unroll") for (int i = 0; i < 4; ++i) e[i] = cexp2(S[b + i]); PIN(e); SB(); } while (0)
#define EXP4P(P, S, b) do { P[0] = pk2(e[0], e[1]); P[1] = pk2(e[2], e[3]); \
    P[2] = pk2(cexp2(S[b]), cexp2(S[b + 1])); P[3] = pk2(cexp2(S[b + 2]), cexp2(S[b + 3])); PIN(P); SB(); } while (0)
#define THEAD(j, SC, SN, KN, KL, VL0, VL1) do { \
    EXP4(SC, 0); \
    SN = MFMA(KN, qf, XC(SN)); PIN(SN); SB(); \
    KL = lds_read16(kp + (j + 2) * 1024); VL0 = lds_read16(vp + (j + 1) * 1024); VL1 = lds_read16(vp + (j + 1) * 1024 + 512); SB(); \
    EXP4P(p0, SC, 4); } while (0)
#define TTAIL(SC, SN, KO, VU0, VU1) do { \
    o = MFMA(VU0, BC(p0), o); PIN(o); SB(); \
    EXP4(SC, 8); \
    XU(SN = MFMA(KO, nqf, SN); PIN(SN); SB();) \
    EXP4P(p1, SC, 12); \
    o = MFMA(VU1, BC(p1), o); PIN(o); SB(); } while (0)
  f32x4 e;
  bf16x8 nqf;
  { u32x4 t = __builtin_bit_cast(u32x4, qf); t[0] ^= 0x80008000u; t[1] ^= 0x80008000u; t[2] ^= 0x80008000u; t[3] ^= 0x80008000u; nqf = BC(t); }
  int it = 0;
#pragma unroll 1
  for (;;) {
    lds_cptr kp = kp0 + it * 4096, vp = vp0 + it * 4 * vinc;
    bf16x8 k0 = lds_read16(kp), k1 = lds_read16(kp + 1024), k2, k3 = k0;
    bf16x8 vA0 = lds_read16(vp), vA1 = lds_read16(vp + 512), vB0, vB1;
    f32x16 negm = splat16(nm); PIN(negm);
    f32x16 sA = MFMA(k0, qf, splat16(nm)), sB = sA;
    u32x4 p0, p1;
    THEAD(0, sA, sB, k1, k2, vB0, vB1);
    bool trig;
#pragma unroll 1
    do {
      TTAIL(sA, sB, k3, vA0, vA1);
      THEAD(1, sB, sA, k2, k3, vA0, vA1); TTAIL(sB, sA, k0, vB0, vB1);
      THEAD(2, sA, sB, k3, k0, vB0, vB1); TTAIL(sA, sB, k1, vA0, vA1);
      THEAD(3, sB, sA, k0, k1, vA0, vA1); TTAIL(sB, sA, k2, vB0, vB1);
      kp += 4096; vp += 4 * vinc; ++it;
      if ((it & 1) == 0 && it < 14) k2_need(lds, (it >> 1) + 2);
      {
        volatile __attribute__((address_space(3))) int* pg = (volatile __attribute__((address_space(3))) int*)(lds + K2_CNT + 64);
        if (lane == 0) pg[wave] = it;
        const int other = __builtin_amdgcn_readfirstlane(pg[wave ^ 4]);
        if (it > other) __builtin_amdgcn_s_setprio(1); else __builtin_amdgcn_s_setprio(2);
      }
      THEAD(0, sA, sB, k1, k2, vB0, vB1);
      trig = __any(o[8] >= 1.f);
    } while (it < K2_ITERS && !trig);
    if (__builtin_expect(!trig, 1)) break;
    k2_fix(o, nm, qf, kp0, vp0, vinc, 4 * it - 4, h);
    if (it >= K2_ITERS) break;
  }
  asm volatile("s_nop 15" : "+v"(o));
  const float il = 1.f / swap_add(h == 0 ? o[8] : 0.f);
  u32x4 ov = {pk2(mul_s(o[0], il), mul_s(o[1], il)), pk2(mul_s(o[2], il), mul_s(o[3], il)), pk2(mul_s(o[4], il), mul_s(o[5], il)), pk2(mul_s(o[6], il), mul_s(o[7], il))};
  st16(ws + WS_A + ((size_t)mt * KST3 + head) * 1024 + lane * 16, ov);
}

DI void k2_ffn(char* __restrict__ ws) {
  const int lane = threadIdx.x & 63;
  const int fw = __builtin_amdgcn_readfirstlane(threadIdx.x >> 6) - 8;
  const int bid = blockIdx.x;
  const int sp = (bid & 7) + 8 * fw, mp = bid >> 3;
  if (sp < 43) {
    const __amdgpu_buffer_rsrc_t rs = __builtin_amdgcn_make_buffer_rsrc(ws, 0, 0x7fffffff, 0x00020000);
    const int xo = (int)WS_X + mp * 65536, wo = (int)WS_G + sp * 65536, l16 = lane * 16;
    f32x16 acc[2][2] = {{f32x16{}, f32x16{}}, {f32x16{}, f32x16{}}};
#define FF_PF 3
    constexpr int PF = FF_PF;
    u32x4 a0[PF], a1[PF], b0[PF], b1[PF];
#define BLD(off) __builtin_amdgcn_raw_buffer_load_b128(rs, l16, (off), 0)
#define FF_LOAD(i, k) do { a0[i] = BLD(xo + (k) * 1024); a1[i] = BLD(xo + 32768 + (k) * 1024); \
    b0[i] = BLD(wo + (k) * 1024); b1[i] = BLD(wo + 32768 + (k) * 1024); } while (0)
#pragma unroll
    for (int i = 0; i < PF; ++i) FF_LOAD(i, i);
#pragma unroll
    for (int k = 0; k < 32; ++k) {
      const int i = k % PF;
      acc[0][0] = MFMA(BC(b0[i]), BC(a0[i]), acc[0][0]); acc[0][1] = MFMA(BC(b1[i]), BC(a0[i]), acc[0][1]);
      acc[1][0] = MFMA(BC(b0[i]), BC(a1[i]), acc[1][0]); acc[1][1] = MFMA(BC(b1[i]), BC(a1[i]), acc[1][1]);
      PIN(acc[1][1]); SB();
      if (k + PF < 32) FF_LOAD(i, k + PF);
      SB();
    }
    asm volatile("s_nop 15\n\ts_nop 15" : "+v"(acc[0][0]), "+v"(acc[0][1]), "+v"(acc[1][0]), "+v"(acc[1][1]));
    SB();
#pragma unroll
    for (int mi = 0; mi < 2; ++mi)
#pragma unroll
      for (int s = 0; s < 2; ++s) {
        float v[8];
#pragma unroll
        for (int j = 0; j < 8; ++j) {
          const float g = acc[mi][s][j], u = acc[mi][s][8 + j];
          float d = __builtin_amdgcn_exp2f(mul_s(g, -1.4426950408889634f)); add_s(d, 1.f);
          v[j] = mul_s(mul_s(g, __builtin_amdgcn_rcpf(d)), u);
        }
        u32x4 o = {pk2(v[0], v[1]), pk2(v[2], v[3]), pk2(v[4], v[5]), pk2(v[6], v[7])};
        st16(ws + WS_A + ((size_t)(mp * 2 + mi) * KST3 + 32 + sp * 2 + s) * 1024 + lane * 16, o);
      }
  }
}

__global__ __launch_bounds__(1024, 4) void k2_kernel(char* __restrict__ ws, const u16* __restrict__ wo, const u16* __restrict__ wd) {
  __shared__ __attribute__((aligned(16))) char lds[K2_LDS];
  if (threadIdx.x < 32) *(unsigned*)(lds + K2_CNT + threadIdx.x * 4) = 0u;
  __syncthreads();
  const int wv = __builtin_amdgcn_readfirstlane(threadIdx.x >> 6);
  if (wv < 8) k2_attn(lds, ws); else if (wv < 14) k2_ffn(ws); else k2_loader(lds, ws, wv - 14, wo, wd);
}

constexpr int K3_ROWB = 64 * 4 + 16;
#define K3_PF 6
#define K3_ROT 0
__global__ __launch_bounds__(256, 1) void k3_kernel(const u16* __restrict__ x, char* __restrict__ ws, u16* __restrict__ out) {
  __shared__ __attribute__((aligned(16))) char lds[4 * 64 * K3_ROWB];
  const int tid = threadIdx.x, lane = tid & 63, r = lane & 31, h = lane >> 5;
  const int wave = __builtin_amdgcn_readfirstlane(tid >> 6);
  const int bid = blockIdx.x;
  const int mb = (bid & 7) * 4 + ((bid >> 3) & 3), nb = bid >> 5;
  const int kq = (wave + (K3_ROT)) & 3;
  const int k0 = kq * 30 - (kq > 2 ? 1 : 0), nk = (kq < 2) ? 30 : 29;
  const __amdgpu_buffer_rsrc_t rs = __builtin_amdgcn_make_buffer_rsrc(ws, 0, 0x7fffffff, 0x00020000);
  const int ao = (int)WS_A + (mb * 2 * KST3 + k0) * 1024, wo = (int)WS_W + (k0 * 16 + nb * 2) * 1024, l16 = lane * 16;
  const int m = tid >> 2, n0 = (tid & 3) * 16;
  const size_t go = (size_t)(mb * 64 + m) * D_ + nb * 64 + n0;
  f32x16 acc[2][2] = {{f32x16{}, f32x16{}}, {f32x16{}, f32x16{}}};
  constexpr int PF = K3_PF;
  u32x4 a0[PF], a1[PF], b0[PF], b1[PF];
  const int klast = nk - 1;
#define K3_LOAD(i, k) do { const int kk_ = ((k) < 29) ? (k) : klast; \
    a0[i] = BLD(ao + kk_ * 1024); a1[i] = BLD(ao + KST3 * 1024 + kk_ * 1024); \
    b0[i] = BLD(wo + kk_ * 16384); b1[i] = BLD(wo + kk_ * 16384 + 1024); } while (0)
#pragma unroll
  for (int i = 0; i < PF; ++i) K3_LOAD(i, i);
#pragma unroll
  for (int k = 0; k < 30; ++k) {
    const int i = k % PF;
    if (k < 29 || nk == 30) {
      acc[0][0] = MFMA(BC(b0[i]), BC(a0[i]), acc[0][0]); acc[0][1] = MFMA(BC(b1[i]), BC(a0[i]), acc[0][1]);
      acc[1][0] = MFMA(BC(b0[i]), BC(a1[i]), acc[1][0]); acc[1][1] = MFMA(BC(b1[i]), BC(a1[i]), acc[1][1]);
    }
    if (k + PF < 30) K3_LOAD(i, k + PF);
    SB();
  }
  const u32x4 x0 = *(const u32x4*)(x + go), x1 = *(const u32x4*)(x + go + 8);
  char* pw = lds + wave * 64 * K3_ROWB;
#pragma unroll
  for (int i = 0; i < 2; ++i)
#pragma unroll
    for (int j = 0; j < 2; ++j)
#pragma unroll
      for (int g = 0; g < 4; ++g) {
        f32x4 v = {acc[i][j][4 * g], acc[i][j][4 * g + 1], acc[i][j][4 * g + 2], acc[i][j][4 * g + 3]};
        *(f32x4*)(pw + (i * 32 + r) * K3_ROWB + (j * 32 + 8 * g + 4 * h) * 4) = v;
      }
  __syncthreads();
  float s[16];
#pragma unroll
  for (int j = 0; j < 4; ++j) {
    s[2 * j] = lo16(x0[j]); s[2 * j + 1] = hi16(x0[j]);
    s[8 + 2 * j] = lo16(x1[j]); s[8 + 2 * j + 1] = hi16(x1[j]);
  }
#pragma unroll
  for (int w = 0; w < 4; ++w)
#pragma unroll
    for (int q = 0; q < 4; ++q) {
      const f32x4 v = *(const f32x4*)(lds + w * 64 * K3_ROWB + m * K3_ROWB + (n0 + 4 * q) * 4);
#pragma unroll
      for (int j = 0; j < 4; ++j) s[4 * q + j] += v[j];
    }
  u32x4 o0 = {pk2(s[0], s[1]), pk2(s[2], s[3]), pk2(s[4], s[5]), pk2(s[6], s[7])};
  u32x4 o1 = {pk2(s[8], s[9]), pk2(s[10], s[11]), pk2(s[12], s[13]), pk2(s[14], s[15])};
  *(u32x4*)(out + go) = o0; *(u32x4*)(out + go + 8) = o1;
}

extern "C" void kernel_launch(void* const* d_in, const int* in_sizes, int n_in, void* d_out, int out_size, void* d_ws, size_t ws_size,
                              hipStream_t stream) {
  const u16* x = (const u16*)d_in[0];
  const u16* wqkv = (const u16*)d_in[1];
  const u16* wo = (const u16*)d_in[2];
  const u16* wg = (const u16*)d_in[4];
  const u16* wu = (const u16*)d_in[5];
  const u16* wd = (const u16*)d_in[6];
  char* ws = (char*)d_ws;
  k1_kernel<<<256, 768, 0, stream>>>(x, wqkv, wg, wu, ws);
  k2_kernel<<<256, 1024, 0, stream>>>(ws, wo, wd);
  k3_kernel<<<256, 256, 0, stream>>>(x, ws, (u16*)d_out);
}
```

```cpp
#include <hip/hip_runtime.h>
#include <hip/hip_bf16.h>

typedef short bf16x8 __attribute__((ext_vector_type(8)));
typedef short s16x4 __attribute__((ext_vector_type(4)));
typedef float f32x16 __attribute__((ext_vector_type(16)));
typedef float f32x4 __attribute__((ext_vector_type(4)));
typedef unsigned u32x4 __attribute__((ext_vector_type(4)));
typedef unsigned u32x2 __attribute__((ext_vector_type(2)));
typedef unsigned short u16;
typedef __attribute__((address_space(3))) const char* lds_cptr;
typedef __bf16 bf16x2_t __attribute__((ext_vector_type(2)));
typedef float f32x2_t __attribute__((ext_vector_type(2)));

#define DI __device__ __forceinline__
#define MFMA(a, b, c) __builtin_amdgcn_mfma_f32_32x32x16_bf16((a), (b), (c), 0, 0, 0)
#define BC(x) __builtin_bit_cast(bf16x8, x)
#define WAITV(n) asm volatile("s_waitcnt vmcnt(" #n ")" ::: "memory")
#define BAR() __builtin_amdgcn_s_barrier()
#define SB() __builtin_amdgcn_sched_barrier(0)
#define PIN(x) asm volatile("" : "+v"(x))

constexpr int S_ = 2048, D_ = 512, F_ = 1376, NH = 32;
constexpr int KST3 = 118;
constexpr size_t WS_Q = 0;
constexpr size_t WS_K = WS_Q + (size_t)NH * 64 * 1024;
constexpr size_t WS_V = WS_K + (size_t)NH * 64 * 1024;
constexpr size_t WS_A = WS_V + (size_t)NH * 64 * 1024;
constexpr size_t WS_W = WS_A + (size_t)64 * KST3 * 1024;
constexpr size_t WS_X = WS_W + (size_t)KST3 * 16 * 1024;
constexpr size_t WS_G = WS_X + (size_t)64 * 32 * 1024;

DI unsigned pk2(float a, float b) { f32x2_t v = {a, b}; return __builtin_bit_cast(unsigned, __builtin_convertvector(v, bf16x2_t)); }
DI float swap_add(float v) {
  auto rr = __builtin_amdgcn_permlane32_swap(__float_as_uint(v), __float_as_uint(v), false, false);
  return __uint_as_float(rr[0]) + __uint_as_float(rr[1]);
}
DI float swap_max(float v) {
  auto rr = __builtin_amdgcn_permlane32_swap(__float_as_uint(v), __float_as_uint(v), false, false);
  return __builtin_fmaxf(__uint_as_float(rr[0]), __uint_as_float(rr[1]));
}
DI s16x4 tr_read(lds_cptr p) {
  typedef short v4i16_t __attribute__((ext_vector_type(4)));
  return __builtin_bit_cast(s16x4, __builtin_amdgcn_ds_read_tr16_b64_v4i16((__attribute__((address_space(3))) v4i16_t*)p));
}
DI bf16x8 lds_read16(lds_cptr p) { return *(const __attribute__((address_space(3))) bf16x8*)p; }
DI void glds16(const void* g, char* lds) {
  __builtin_amdgcn_global_load_lds((const __attribute__((address_space(1))) void*)g, (__attribute__((address_space(3))) void*)lds, 16, 0, 0);
}
DI void add_s(float& a, float b) { a = a + b; asm("" : "+v"(a)); }
DI void fmac_s(float& a, float b, float c) { a = __builtin_fmaf(b, c, a); asm("" : "+v"(a)); }
DI float mul_s(float a, float b) { float r = a * b; asm("" : "+v"(r)); return r; }
#define ST_MODE 0
DI void st16(void* p, u32x4 v) {
  *(u32x4*)p = v;
}
DI float lo16(unsigned u) { return __uint_as_float(u << 16); }
DI float hi16(unsigned u) { return __uint_as_float(u & 0xffff0000u); }

#define ST1(i)

constexpr int K1_AB = 16384;
constexpr int K1_SLOT = 32768;
constexpr int K1_NSLOT = 5;
constexpr int K1_LDS = K1_NSLOT * K1_SLOT;
constexpr int K1_XS = 0;
constexpr int K1_XP = 256;
constexpr int K1_CX = 2048;

DI void k1_helper(char* lds, int panel, int col0, int rot, const u16* __restrict__ x, const u16* __restrict__ w) {
  const int lane = threadIdx.x & 63;
  const int hw = __builtin_amdgcn_readfirstlane(threadIdx.x >> 6) - 8;
  const u16 *sa, *sb;
  {
    const int row = hw * 32 + (lane >> 3), ch = (lane & 7) ^ ((row >> 1) & 3);
    sa = x + (size_t)(panel * 128 + row) * D_ + ch * 8;
    const int kr = hw * 16 + (lane >> 4), pc = (lane & 15) ^ ((kr & 3) << 2);
    sb = w + (size_t)kr * (3 * D_) + col0 + pc * 8;
  }
  const int a4 = ((lane & 7) ^ (((lane >> 4) & 3) | 4)) * 8 - ((lane & 7) ^ ((lane >> 4) & 3)) * 8;
  auto stage = [&](int kc) {
    char* d = lds + (kc % K1_NSLOT) * K1_SLOT + hw * 4096;
    const int k0 = ((kc + rot) & 7) * 64;
#pragma unroll
    for (int i = 0; i < 4; ++i) glds16(sa + k0 + i * 8 * D_ + ((i & 1) ? a4 : 0), d + i * 1024);
#pragma unroll
    for (int i = 0; i < 4; ++i) glds16(sb + (size_t)(k0 + i * 4) * (3 * D_), d + K1_AB + i * 1024);
  };
  stage(0); stage(1);
  WAITV(8);
  BAR();
  stage(2); stage(3);
#pragma unroll 1
  for (int kc = 0; kc < 8; ++kc) {
    if (kc < 5) WAITV(16); else if (kc == 5) WAITV(8); else WAITV(0);
    BAR();
    if (kc + 4 < 8) stage(kc + 4);
  }
  float* sc = (float*)lds;
  __syncthreads();
  if (hw < 2) {
    const int i = hw * 64 + lane;
    const float* p = sc + K1_XP + i;
    const float mean = ((p[0] + p[256]) + (p[512] + p[768])) * (1.f / D_);
    const float var = __builtin_fmaxf(((p[128] + p[384]) + (p[640] + p[896])) * (1.f / D_) - mean * mean, 0.f);
    sc[K1_XS + i] = mean; sc[K1_XS + 128 + i] = __builtin_amdgcn_rsqf(var + 1e-5f);
  }
  __syncthreads();
}

template <int TYPE, int CW>
DI void k1_gemm(char* lds, int panel, int st0, char* __restrict__ ws) {
  const int tid = threadIdx.x, lane = tid & 63, r = lane & 31, h = lane >> 5;
  const int mw = __builtin_amdgcn_readfirstlane(tid >> 6) & 1;
  f32x16 acc[2] = {f32x16{}, f32x16{}}, acs = f32x16{};
  const u32x4 ones_u = {0x3f803f80u, 0x3f803f80u, 0x3f803f80u, 0x3f803f80u};
  const bf16x8 ones = BC(ones_u);
  const int arow = mw * 64 + r;
  const int aswz = (arow >> 1) & 7;
  lds_cptr ab[4];
#pragma unroll
  for (int ks = 0; ks < 4; ++ks) ab[ks] = (lds_cptr)lds + arow * 128 + (((2 * ks + h) ^ aswz) << 4);
  const lds_cptr bb = (lds_cptr)lds + K1_AB + (8 * h + ((lane & 15) >> 2)) * 256 + (CW ^ ((lane & 15) >> 2)) * 64 + ((lane >> 4) & 1) * 32 + (lane & 3) * 8;
  bf16x8 af[2][2][2] = {}; s16x4 bl[2][2] = {}, bh[2][2] = {};
  float sm[2][2] = {{0.f, 0.f}, {0.f, 0.f}}, sq[2][2] = {{0.f, 0.f}, {0.f, 0.f}};
#define K1_FRAGS(buf, soff) do { __builtin_amdgcn_s_waitcnt(0xC07F); \
    _Pragma("unroll") for (int ks = 0; ks < 2; ++ks) { const lds_cptr a_ = ab[2 * (buf) + ks] + (soff); \
      af[buf][ks][0] = lds_read16(a_); af[buf][ks][1] = lds_read16(a_ + 4096); \
      bl[buf][ks] = tr_read(bb + (soff) + (2 * (buf) + ks) * 4096); bh[buf][ks] = tr_read(bb + (soff) + (2 * (buf) + ks) * 4096 + 1024); } SB(); } while (0)
#define K1_MMA(buf) do { _Pragma("unroll") for (int ks = 0; ks < 2; ++ks) { \
    const bf16x8 bf = __builtin_shufflevector(bl[buf][ks], bh[buf][ks], 0, 1, 2, 3, 4, 5, 6, 7); \
    _Pragma("unroll") for (int mi = 0; mi < 2; ++mi) { \
      if (TYPE == 1) acc[mi] = MFMA(af[buf][ks][mi], bf, acc[mi]); else acc[mi] = MFMA(bf, af[buf][ks][mi], acc[mi]); \
      const unsigned u = __builtin_bit_cast(u32x4, af[buf][ks][mi])[CW]; const float lo = lo16(u), hi = hi16(u); \
      add_s(sm[mi][0], lo); add_s(sm[mi][1], hi); fmac_s(sq[mi][0], lo, lo); fmac_s(sq[mi][1], hi, hi); \
      asm volatile("" : "+v"(sm[mi][0]), "+v"(sm[mi][1]), "+v"(sq[mi][0]), "+v"(sq[mi][1])); SB(); } } \
    if (mw == (buf)) {        \
      _Pragma("unroll") for (int ks = 0; ks < 2; ++ks) { const bf16x8 bf = __builtin_shufflevector(bl[buf][ks], bh[buf][ks], 0, 1, 2, 3, 4, 5, 6, 7); \
        if (TYPE == 1) acs = MFMA(ones, bf, acs); else acs = MFMA(bf, ones, acs); } } SB(); } while (0)
  ST1(0);
  BAR();
  ST1(1);
  K1_FRAGS(0, 0);
  int scur = 0, snext = K1_SLOT;
#pragma unroll 1
  for (int kc = 0; kc < 8; ++kc) {
    BAR();
    K1_FRAGS(1, scur); K1_MMA(0);
    K1_FRAGS(0, snext); K1_MMA(1);
    scur = snext; snext = (snext == (K1_NSLOT - 1) * K1_SLOT) ? 0 : snext + K1_SLOT;
  }
  ST1(17);
#pragma unroll
  for (int mi = 0; mi < 2; ++mi) {
    const float t = swap_add(sm[mi][0] + sm[mi][1]), u = swap_add(sq[mi][0] + sq[mi][1]);
    float* xp = (float*)lds + K1_XP + CW * 256 + mw * 64 + mi * 32 + r;
    if (h == 0) { xp[0] = t; xp[128] = u; }
  }
  float* cx = (float*)lds + K1_CX + CW * 2048 + lane * 4;
#pragma unroll
  for (int g = 0; g < 4; ++g) { f32x4 v = {acs[4 * g], acs[4 * g + 1], acs[4 * g + 2], acs[4 * g + 3]}; *(f32x4*)(cx + mw * 1024 + g * 256) = v; }
  __syncthreads();
#pragma unroll
  for (int g = 0; g < 4; ++g) { const f32x4 v = *(const f32x4*)(cx + (mw ^ 1) * 1024 + g * 256);
#pragma unroll
    for (int j = 0; j < 4; ++j) acs[4 * g + j] += v[j]; }
  __syncthreads();
  const float* xs = (const float*)lds + K1_XS;
  const int st = st0 + CW;
#pragma unroll
  for (int mi = 0; mi < 2; ++mi) {
    const int mt = panel * 4 + mw * 2 + mi;
    const int lr = mw * 64 + mi * 32;
    if (TYPE == 0) {
      const float mean = xs[lr + r], rstd = xs[128 + lr + r];
      const float sc = (st < 16) ? rstd * (0.25f * 1.4426950408889634f) : rstd;
      char* dstp = ws + ((st < 16) ? WS_Q : WS_K) + ((size_t)((st & 15) * 2) * 64 + mt) * 1024 + lane * 16;
#pragma unroll
      for (int hd = 0; hd < 2; ++hd) {
        float v[8];
#pragma unroll
        for (int j = 0; j < 8; ++j) v[j] = (acc[mi][8 * hd + j] - mean * acs[8 * hd + j]) * sc;
        u32x4 o = {pk2(v[0], v[1]), pk2(v[2], v[3]), pk2(v[4], v[5]), pk2(v[6], v[7])};
        st16(dstp + (size_t)hd * 64 * 1024, o);
      }
    } else {
      const float c = acs[0];
      char* dstp = ws + WS_V + ((size_t)((st - 32) * 2 + (r >> 4)) * 64 + mt) * 1024 + h * 256 + (r & 15) * 16;
#pragma unroll
      for (int ks = 0; ks < 2; ++ks) {
        const f32x4 m0 = *(const f32x4*)(xs + lr + 16 * ks + 4 * h), m1 = *(const f32x4*)(xs + lr + 16 * ks + 8 + 4 * h);
        const f32x4 r0 = *(const f32x4*)(xs + 128 + lr + 16 * ks + 4 * h), r1 = *(const f32x4*)(xs + 128 + lr + 16 * ks + 8 + 4 * h);
        float v[8];
#pragma unroll
        for (int j = 0; j < 4; ++j) { v[j] = (acc[mi][8 * ks + j] - m0[j] * c) * r0[j]; v[4 + j] = (acc[mi][8 * ks + 4 + j] - m1[j] * c) * r1[j]; }
        u32x4 o = {pk2(v[0], v[1]), pk2(v[2], v[3]), pk2(v[4], v[5]), pk2(v[6], v[7])};
        st16(dstp + ks * 512, o);
      }
    }
  }
  ST1(18);
}

#define K1_ROT 0
__global__ __launch_bounds__(768, 3) void k1_kernel(const u16* __restrict__ x, const u16* __restrict__ wqkv,
                                                    const u16* __restrict__ wg, const u16* __restrict__ wu, char* __restrict__ ws) {
  __shared__ __attribute__((aligned(16))) char lds[K1_LDS];
  const int bid = blockIdx.x;
  const int xcd = bid & 7, idx = bid >> 3;
  const int wave = __builtin_amdgcn_readfirstlane(threadIdx.x >> 6);
  const int lane = threadIdx.x & 63, c = lane & 31, h = lane >> 5;
  if (idx < 24) {
    const int panel = (xcd & 3) * 4 + (idx & 3), grp = (xcd >> 2) * 6 + (idx >> 2);
    if (wave >= 8) k1_helper(lds, panel, grp * 128, K1_ROT, x, wqkv);
    else if (grp < 8) {
      switch (wave >> 1) {
        case 0: k1_gemm<0, 0>(lds, panel, grp * 4, ws); break; case 1: k1_gemm<0, 1>(lds, panel, grp * 4, ws); break;
        case 2: k1_gemm<0, 2>(lds, panel, grp * 4, ws); break; default: k1_gemm<0, 3>(lds, panel, grp * 4, ws); break;
      }
    } else {
      switch (wave >> 1) {
        case 0: k1_gemm<1, 0>(lds, panel, grp * 4, ws); break; case 1: k1_gemm<1, 1>(lds, panel, grp * 4, ws); break;
        case 2: k1_gemm<1, 2>(lds, panel, grp * 4, ws); break; default: k1_gemm<1, 3>(lds, panel, grp * 4, ws); break;
      }
    }
  } else {
    const int pb = (idx - 24) * 8 + xcd;
    if (wave < 4) {
      u32x4 v[8];
#pragma unroll
      for (int i = 0; i < 8; ++i) v[i] = *(const u32x4*)(x + (size_t)(pb * 32 + wave * 8 + i) * D_ + lane * 8);
      float f[8][8], sm[8];
#pragma unroll
      for (int i = 0; i < 8; ++i) {
#pragma unroll
        for (int j = 0; j < 4; ++j) { f[i][2 * j] = lo16(v[i][j]); f[i][2 * j + 1] = hi16(v[i][j]); }
        sm[i] = ((f[i][0] + f[i][1]) + (f[i][2] + f[i][3])) + ((f[i][4] + f[i][5]) + (f[i][6] + f[i][7]));
      }
#pragma unroll
      for (int d = 1; d < 64; d <<= 1)
#pragma unroll
        for (int i = 0; i < 8; ++i) sm[i] += __shfl_xor(sm[i], d);
#pragma unroll
      for (int i = 0; i < 8; ++i) {
        const float mean = sm[i] * (1.f / D_);
        float q = 0.f;
#pragma unroll
        for (int j = 0; j < 8; ++j) { f[i][j] -= mean; q += f[i][j] * f[i][j]; }
        sm[i] = q;
      }
#pragma unroll
      for (int d = 1; d < 64; d <<= 1)
#pragma unroll
        for (int i = 0; i < 8; ++i) sm[i] += __shfl_xor(sm[i], d);
#pragma unroll
      for (int i = 0; i < 8; ++i) {
        const float rstd = __builtin_amdgcn_rsqf(sm[i] * (1.f / D_) + 1e-5f);
        u32x4 o;
#pragma unroll
        for (int j = 0; j < 4; ++j) o[j] = pk2(f[i][2 * j] * rstd, f[i][2 * j + 1] * rstd);
        *(u32x4*)(ws + WS_X + ((size_t)pb * 32 + (lane >> 1)) * 1024 + ((lane & 1) * 32 + wave * 8 + i) * 16) = o;
      }
    } else {
      constexpr int NG = 86 * 32;
      const int wv = pb * 8 + wave - 4;
      char* wl = lds + wave * 8192;
      const int kr = lane >> 2, p = lane & 3;
#pragma unroll
      for (int i = 0; i < 6; ++i) {
        int f = wv + i * 512; if (f > NG - 1) f = NG - 1;
        const int st = f >> 5, ks = f & 31;
        glds16(((p < 2) ? wg : wu) + (size_t)(ks * 16 + kr) * F_ + st * 16 + (p & 1) * 8, wl + i * 1024);
      }
      WAITV(0);
      const lds_cptr tb = (lds_cptr)wl + (8 * h + ((lane & 15) >> 2)) * 64 + ((lane >> 4) & 1) * 32 + (lane & 3) * 8;
#pragma unroll
      for (int i = 0; i < 6; ++i) {
        const int f = wv + i * 512;
        const bf16x8 o = __builtin_shufflevector(tr_read(tb + i * 1024), tr_read(tb + i * 1024 + 256), 0, 1, 2, 3, 4, 5, 6, 7);
        if (f < NG) st16(ws + WS_G + (size_t)f * 1024 + lane * 16, __builtin_bit_cast(u32x4, o));
      }
    }
  }
}

constexpr int K2_V = 65536, K2_C = 131072, K2_Q = K2_C + 8192, K2_T = K2_Q + 8192, K2_CNT = K2_T + 8192, K2_LDS = K2_CNT + 128;
#define K2_HEAD 102.f
#define K2_INIT 8
constexpr float HEAD = K2_HEAD;
#define K2_ITERS 16

DI float cexp2(float x) { return __builtin_amdgcn_fmed3f(__builtin_amdgcn_exp2f(x), 0.f, 1.f); }
DI void exp_pack(const f32x16& s, u32x4& p0, u32x4& p1) {
#pragma unroll
  for (int i = 0; i < 4; ++i) { p0[i] = pk2(cexp2(s[2 * i]), cexp2(s[2 * i + 1])); p1[i] = pk2(cexp2(s[8 + 2 * i]), cexp2(s[9 + 2 * i])); }
}
DI float max16(const f32x16& s) {
  float m = __builtin_fmaxf(__builtin_fmaxf(s[0], s[1]), s[2]);
#pragma unroll
  for (int i = 3; i < 15; i += 2) m = __builtin_fmaxf(__builtin_fmaxf(m, s[i]), s[i + 1]);
  return __builtin_fmaxf(m, s[15]);
}
DI f32x16 splat16(float v) { f32x16 r;
#pragma unroll
  for (int i = 0; i < 16; ++i) r[i] = v;
  return r; }

DI void k2_fix(f32x16& o, float& nm, const bf16x8 qf, lds_cptr kp0, lds_cptr vp0, int vinc, int t0, int h) {
  float sm = -3.0e38f;
#pragma unroll 1
  for (int t = t0; t < t0 + 4; ++t) {
    const f32x16 s = MFMA(lds_read16(kp0 + t * 1024), qf, splat16(nm));
    sm = __builtin_fmaxf(sm, max16(s));
    u32x4 p0, p1; exp_pack(s, p0, p1);
#pragma unroll
    for (int i = 0; i < 4; ++i) { p0[i] ^= 0x80008000u; p1[i] ^= 0x80008000u; }
    o = MFMA(lds_read16(vp0 + t * vinc), BC(p0), o);
    o = MFMA(lds_read16(vp0 + t * vinc + 512), BC(p1), o);
  }
  sm = swap_max(sm);
  const float lo = swap_add(h == 0 ? o[8] : 0.f);
  sm = __builtin_fmaxf(sm, __builtin_amdgcn_logf(__builtin_fmaxf(lo, 1e-37f)));
  const float dl = __builtin_fmaxf(sm + HEAD, 0.f);
  const float f = __builtin_amdgcn_exp2f(-dl);
#pragma unroll
  for (int i = 0; i < 16; ++i) o[i] *= f;
  nm -= dl;
#pragma unroll 1
  for (int t = t0; t < t0 + 4; ++t) {
    const f32x16 s = MFMA(lds_read16(kp0 + t * 1024), qf, splat16(nm));
    u32x4 p0, p1; exp_pack(s, p0, p1);
    o = MFMA(lds_read16(vp0 + t * vinc), BC(p0), o);
    o = MFMA(lds_read16(vp0 + t * vinc + 512), BC(p1), o);
  }
}
DI void k2_sync(char* lds, int target) {
  volatile __attribute__((address_space(3))) unsigned* cnt = (volatile __attribute__((address_space(3))) unsigned*)(lds + K2_CNT);
  if ((threadIdx.x & 63) == 0) __hip_atomic_fetch_add((__attribute__((address_space(3))) unsigned*)(lds + K2_CNT), 1u, __ATOMIC_RELAXED, __HIP_MEMORY_SCOPE_WORKGROUP);
  while ((int)__builtin_amdgcn_readfirstlane(*cnt) < target) __builtin_amdgcn_s_sleep(1);
  asm volatile("" ::: "memory");
}

DI void k2_need(char* lds, int n) {
  volatile __attribute__((address_space(3))) int* pg = (volatile __attribute__((address_space(3))) int*)(lds + K2_CNT + 16);
  while (__builtin_amdgcn_readfirstlane(pg[0]) < n || __builtin_amdgcn_readfirstlane(pg[1]) < n) __builtin_amdgcn_s_sleep(1);
  asm volatile("" ::: "memory");
}
DI void k2_loader(char* lds, char* __restrict__ ws, int which, const u16* __restrict__ wo, const u16* __restrict__ wd) {
  const int lane = threadIdx.x & 63, bid = blockIdx.x;
  const int head = (bid & 7) * 4 + ((bid >> 3) & 3);
  const char* g = ws + (which ? WS_V : WS_K) + (size_t)head * 65536 + lane * 16;
  char* l = lds + (which ? K2_V : 0);
  volatile __attribute__((address_space(3))) int* pg = (volatile __attribute__((address_space(3))) int*)(lds + K2_CNT + 16) + which;
  __builtin_amdgcn_s_setprio(3);
  const int c0 = which ? 0 : 1;
#pragma unroll 1
  for (int c = c0; c < 10; ++c) {
    if (c < 8) {
#pragma unroll
      for (int i = 0; i < 8; ++i) glds16(g + c * 8192 + i * 1024, l + c * 8192 + i * 1024);
    }
    if (c - 2 >= c0) {
      if (c < 8) WAITV(16); else if (c == 8) WAITV(8); else WAITV(0);
      if (lane == 0) *pg = c - 1;
    }
  }
  __builtin_amdgcn_s_setprio(0);
  constexpr int NF = KST3 * 16;
  const int wv = bid * 2 + which, kr = lane >> 2, p = lane & 3, h = lane >> 5;
  char* wl = lds + K2_T + which * 4096;
#pragma unroll
  for (int i = 0; i < 4; ++i) {
    int f = wv + i * 512; if (f > NF - 1) f = NF - 1;
    const int ks = f >> 4, nt = f & 15;
    glds16(((ks < 32) ? wo + (size_t)(ks * 16 + kr) * D_ : wd + (size_t)((ks - 32) * 16 + kr) * D_) + nt * 32 + p * 8, wl + i * 1024);
  }
  WAITV(0);
  const lds_cptr tb = (lds_cptr)wl + (4 * h + ((lane & 15) >> 2)) * 64 + ((lane >> 4) & 1) * 32 + (lane & 3) * 8;
#pragma unroll
  for (int i = 0; i < 4; ++i) {
    const int f = wv + i * 512;
    const bf16x8 o = __builtin_shufflevector(tr_read(tb + i * 1024), tr_read(tb + i * 1024 + 512), 0, 1, 2, 3, 4, 5, 6, 7);
    if (f < NF) st16(ws + WS_W + (size_t)f * 1024 + lane * 16, __builtin_bit_cast(u32x4, o));
  }
}
#define TS(i)
DI void k2_attn(char* lds, char* __restrict__ ws) {
  const int tid = threadIdx.x, lane = tid & 63, r = lane & 31, h = lane >> 5;
  const int wave = __builtin_amdgcn_readfirstlane(tid >> 6);
  const int bid = blockIdx.x;
  const int head = (bid & 7) * 4 + ((bid >> 3) & 3), qb = bid >> 5;
  const int mt = qb * 8 + wave;
  glds16(ws + WS_Q + ((size_t)head * 64 + mt) * 1024 + lane * 16, lds + K2_Q + wave * 1024);
  glds16(ws + WS_K + (size_t)head * 65536 + wave * 1024 + lane * 16, lds + wave * 1024);
  {
    const unsigned c = ((tid & 31) == 0) ? 0x3f803f80u : 0u;
    u32x4 z = {c, c, c, c}; *(u32x4*)(lds + K2_C + tid * 16) = z;
  }
  asm volatile("s_waitcnt vmcnt(0) lgkmcnt(0)" ::: "memory");
  k2_sync(lds, 8);
  const bf16x8 qf = lds_read16((lds_cptr)lds + K2_Q + wave * 1024 + lane * 16);
  const lds_cptr kp0 = (lds_cptr)lds + lane * 16;
  const lds_cptr vp0 = (r < 16) ? (lds_cptr)lds + K2_V + h * 256 + r * 16 : (lds_cptr)lds + K2_C + (r == 16 ? 0 : 16);
  const int vinc = (r < 16) ? 1024 : 0;
  f32x16 o = f32x16{};
  float nm;
  {
    const f32x16 z = f32x16{};
    float m = max16(MFMA(lds_read16(kp0), qf, z));
#pragma unroll
    for (int t = 1; t < K2_INIT; ++t) m = __builtin_fmaxf(m, max16(MFMA(lds_read16(kp0 + t * 1024), qf, z)));
    nm = -(swap_max(m) + HEAD);
  }
  TS(0);
  k2_need(lds, 2);
  TS(1);
#define XC(x) negm
#define XU(...)
#define EXP4(S, b) do { _Pragma("unroll") for (int i = 0; i < 4; ++i) e[i] = cexp2(S[b + i]); PIN(e); SB(); } while (0)
#define EXP4P(P, S, b) do { P[0] = pk2(e[0], e[1]); P[1] = pk2(e[2], e[3]); \
    P[2] = pk2(cexp2(S[b]), cexp2(S[b + 1])); P[3] = pk2(cexp2(S[b + 2]), cexp2(S[b + 3])); PIN(P); SB(); } while (0)
#define THEAD(j, SC, SN, KN, KL, VL0, VL1) do { \
    EXP4(SC, 0); \
    SN = MFMA(KN, qf, XC(SN)); PIN(SN); SB(); \
    KL = lds_read16(kp + (j + 2) * 1024); VL0 = lds_read16(vp + (j + 1) * 1024); VL1 = lds_read16(vp + (j + 1) * 1024 + 512); SB(); \
    EXP4P(p0, SC, 4); } while (0)
#define TTAIL(SC, SN, KO, VU0, VU1) do { \
    o = MFMA(VU0, BC(p0), o); PIN(o); SB(); \
    EXP4(SC, 8); \
    XU(SN = MFMA(KO, nqf, SN); PIN(SN); SB();) \
    EXP4P(p1, SC, 12); \
    o = MFMA(VU1, BC(p1), o); PIN(o); SB(); } while (0)
  f32x4 e;
  bf16x8 nqf;
  { u32x4 t = __builtin_bit_cast(u32x4, qf); t[0] ^= 0x80008000u; t[1] ^= 0x80008000u; t[2] ^= 0x80008000u; t[3] ^= 0x80008000u; nqf = BC(t); }
  int it = 0;
#pragma unroll 1
  for (;;) {
    lds_cptr kp = kp0 + it * 4096, vp = vp0 + it * 4 * vinc;
    bf16x8 k0 = lds_read16(kp), k1 = lds_read16(kp + 1024), k2, k3 = k0;
    bf16x8 vA0 = lds_read16(vp), vA1 = lds_read16(vp + 512), vB0, vB1;
    f32x16 negm = splat16(nm); PIN(negm);
    f32x16 sA = MFMA(k0, qf, splat16(nm)), sB = sA;
    u32x4 p0, p1;
    THEAD(0, sA, sB, k1, k2, vB0, vB1);
    bool trig;
#pragma unroll 1
    do {
      TTAIL(sA, sB, k3, vA0, vA1);
      THEAD(1, sB, sA, k2, k3, vA0, vA1); TTAIL(sB, sA, k0, vB0, vB1);
      THEAD(2, sA, sB, k3, k0, vB0, vB1); TTAIL(sA, sB, k1, vA0, vA1);
      THEAD(3, sB, sA, k0, k1, vA0, vA1); TTAIL(sB, sA, k2, vB0, vB1);
      kp += 4096; vp += 4 * vinc; ++it;
      if ((it & 1) == 0 && it < 14) k2_need(lds, (it >> 1) + 2);
      {
        volatile __attribute__((address_space(3))) int* pg = (volatile __attribute__((address_space(3))) int*)(lds + K2_CNT + 64);
        if (lane == 0) pg[wave] = it;
        const int other = __builtin_amdgcn_readfirstlane(pg[wave ^ 4]);
        if (it > other) __builtin_amdgcn_s_setprio(1); else __builtin_amdgcn_s_setprio(2);
      }
      THEAD(0, sA, sB, k1, k2, vB0, vB1);
      trig = __any(o[8] >= 1.f);
    } while (it < K2_ITERS && !trig);
    if (__builtin_expect(!trig, 1)) break;
    k2_fix(o, nm, qf, kp0, vp0, vinc, 4 * it - 4, h);
    if (it >= K2_ITERS) break;
  }
  asm volatile("s_nop 15" : "+v"(o));
  const float il = 1.f / swap_add(h == 0 ? o[8] : 0.f);
  u32x4 ov = {pk2(mul_s(o[0], il), mul_s(o[1], il)), pk2(mul_s(o[2], il), mul_s(o[3], il)), pk2(mul_s(o[4], il), mul_s(o[5], il)), pk2(mul_s(o[6], il), mul_s(o[7], il))};
  st16(ws + WS_A + ((size_t)mt * KST3 + head) * 1024 + lane * 16, ov);
}

DI void k2_ffn(char* __restrict__ ws) {
  const int lane = threadIdx.x & 63;
  const int fw = __builtin_amdgcn_readfirstlane(threadIdx.x >> 6) - 8;
  const int bid = blockIdx.x;
  const int sp = (bid & 7) + 8 * fw, mp = bid >> 3;
  if (sp < 43) {
    const __amdgpu_buffer_rsrc_t rs = __builtin_amdgcn_make_buffer_rsrc(ws, 0, 0x7fffffff, 0x00020000);
    const int xo = (int)WS_X + mp * 65536, wo = (int)WS_G + sp * 65536, l16 = lane * 16;
    f32x16 acc[2][2] = {{f32x16{}, f32x16{}}, {f32x16{}, f32x16{}}};
#define FF_PF 3
    constexpr int PF = FF_PF;
    u32x4 a0[PF], a1[PF], b0[PF], b1[PF];
#define BLD(off) __builtin_amdgcn_raw_buffer_load_b128(rs, l16, (off), 0)
#define FF_LOAD(i, k) do { a0[i] = BLD(xo + (k) * 1024); a1[i] = BLD(xo + 32768 + (k) * 1024); \
    b0[i] = BLD(wo + (k) * 1024); b1[i] = BLD(wo + 32768 + (k) * 1024); } while (0)
#pragma unroll
    for (int i = 0; i < PF; ++i) FF_LOAD(i, i);
#pragma unroll
    for (int k = 0; k < 32; ++k) {
      const int i = k % PF;
      acc[0][0] = MFMA(BC(b0[i]), BC(a0[i]), acc[0][0]); acc[0][1] = MFMA(BC(b1[i]), BC(a0[i]), acc[0][1]);
      acc[1][0] = MFMA(BC(b0[i]), BC(a1[i]), acc[1][0]); acc[1][1] = MFMA(BC(b1[i]), BC(a1[i]), acc[1][1]);
      PIN(acc[1][1]); SB();
      if (k + PF < 32) FF_LOAD(i, k + PF);
      SB();
    }
    asm volatile("s_nop 15\n\ts_nop 15" : "+v"(acc[0][0]), "+v"(acc[0][1]), "+v"(acc[1][0]), "+v"(acc[1][1]));
    SB();
#pragma unroll
    for (int mi = 0; mi < 2; ++mi)
#pragma unroll
      for (int s = 0; s < 2; ++s) {
        float v[8];
#pragma unroll
        for (int j = 0; j < 8; ++j) {
          const float g = acc[mi][s][j], u = acc[mi][s][8 + j];
          float d = __builtin_amdgcn_exp2f(mul_s(g, -1.4426950408889634f)); add_s(d, 1.f);
          v[j] = mul_s(mul_s(g, __builtin_amdgcn_rcpf(d)), u);
        }
        u32x4 o = {pk2(v[0], v[1]), pk2(v[2], v[3]), pk2(v[4], v[5]), pk2(v[6], v[7])};
        st16(ws + WS_A + ((size_t)(mp * 2 + mi) * KST3 + 32 + sp * 2 + s) * 1024 + lane * 16, o);
      }
  }
}

__global__ __launch_bounds__(1024, 4) void k2_kernel(char* __restrict__ ws, const u16* __restrict__ wo, const u16* __restrict__ wd) {
  __shared__ __attribute__((aligned(16))) char lds[K2_LDS];
  if (threadIdx.x < 32) *(unsigned*)(lds + K2_CNT + threadIdx.x * 4) = 0u;
  __syncthreads();
  const int wv = __builtin_amdgcn_readfirstlane(threadIdx.x >> 6);
  if (wv < 8) k2_attn(lds, ws); else if (wv < 14) k2_ffn(ws); else k2_loader(lds, ws, wv - 14, wo, wd);
}

constexpr int K3_ROWB = 64 * 4 + 16;
#define K3_PF 6
#define K3_ROT 0
__global__ __launch_bounds__(256, 1) void k3_kernel(const u16* __restrict__ x, char* __restrict__ ws, u16* __restrict__ out) {
  __shared__ __attribute__((aligned(16))) char lds[4 * 64 * K3_ROWB];
  const int tid = threadIdx.x, lane = tid & 63, r = lane & 31, h = lane >> 5;
  const int wave = __builtin_amdgcn_readfirstlane(tid >> 6);
  const int bid = blockIdx.x;
  const int mb = (bid & 7) * 4 + ((bid >> 3) & 3), nb = bid >> 5;
  const int kq = (wave + (K3_ROT)) & 3;
  const int k0 = kq * 30 - (kq > 2 ? 1 : 0), nk = (kq < 2) ? 30 : 29;
  const __amdgpu_buffer_rsrc_t rs = __builtin_amdgcn_make_buffer_rsrc(ws, 0, 0x7fffffff, 0x00020000);
  const int ao = (int)WS_A + (mb * 2 * KST3 + k0) * 1024, wo = (int)WS_W + (k0 * 16 + nb * 2) * 1024, l16 = lane * 16;
  const int m = tid >> 2, n0 = (tid & 3) * 16;
  const size_t go = (size_t)(mb * 64 + m) * D_ + nb * 64 + n0;
  f32x16 acc[2][2] = {{f32x16{}, f32x16{}}, {f32x16{}, f32x16{}}};
  constexpr int PF = K3_PF;
  u32x4 a0[PF], a1[PF], b0[PF], b1[PF];
  const int klast = nk - 1;
#define K3_LOAD(i, k) do { const int kk_ = ((k) < 29) ? (k) : klast; \
    a0[i] = BLD(ao + kk_ * 1024); a1[i] = BLD(ao + KST3 * 1024 + kk_ * 1024); \
    b0[i] = BLD(wo + kk_ * 16384); b1[i] = BLD(wo + kk_ * 16384 + 1024); } while (0)
#pragma unroll
  for (int i = 0; i < PF; ++i) K3_LOAD(i, i);
#pragma unroll
  for (int k = 0; k < 30; ++k) {
    const int i = k % PF;
    if (k < 29 || nk == 30) {
      acc[0][0] = MFMA(BC(b0[i]), BC(a0[i]), acc[0][0]); acc[0][1] = MFMA(BC(b1[i]), BC(a0[i]), acc[0][1]);
      acc[1][0] = MFMA(BC(b0[i]), BC(a1[i]), acc[1][0]); acc[1][1] = MFMA(BC(b1[i]), BC(a1[i]), acc[1][1]);
    }
    if (k + PF < 30) K3_LOAD(i, k + PF);
    SB();
  }
  const u32x4 x0 = *(const u32x4*)(x + go), x1 = *(const u32x4*)(x + go + 8);
  char* pw = lds + wave * 64 * K3_ROWB;
#pragma unroll
  for (int i = 0; i < 2; ++i)
#pragma unroll
    for (int j = 0; j < 2; ++j)
#pragma unroll
      for (int g = 0; g < 4; ++g) {
        f32x4 v = {acc[i][j][4 * g], acc[i][j][4 * g + 1], acc[i][j][4 * g + 2], acc[i][j][4 * g + 3]};
        *(f32x4*)(pw + (i * 32 + r) * K3_ROWB + (j * 32 + 8 * g + 4 * h) * 4) = v;
      }
  __syncthreads();
  float s[16];
#pragma unroll
  for (int j = 0; j < 4; ++j) {
    s[2 * j] = lo16(x0[j]); s[2 * j + 1] = hi16(x0[j]);
    s[8 + 2 * j] = lo16(x1[j]); s[8 + 2 * j + 1] = hi16(x1[j]);
  }
#pragma unroll
  for (int w = 0; w < 4; ++w)
#pragma unroll
    for (int q = 0; q < 4; ++q) {
      const f32x4 v = *(const f32x4*)(lds + w * 64 * K3_ROWB + m * K3_ROWB + (n0 + 4 * q) * 4);
#pragma unroll
      for (int j = 0; j < 4; ++j) s[4 * q + j] += v[j];
    }
  u32x4 o0 = {pk2(s[0], s[1]), pk2(s[2], s[3]), pk2(s[4], s[5]), pk2(s[6], s[7])};
  u32x4 o1 = {pk2(s[8], s[9]), pk2(s[10], s[11]), pk2(s[12], s[13]), pk2(s[14], s[15])};
  *(u32x4*)(out + go) = o0; *(u32x4*)(out + go + 8) = o1;
}

extern "C" void kernel_launch(void* const* d_in, const int* in_sizes, int n_in, void* d_out, int out_size, void* d_ws, size_t ws_size,
                              hipStream_t stream) {
  const u16* x = (const u16*)d_in[0];
  const u16* wqkv = (const u16*)d_in[1];
  const u16* wo = (const u16*)d_in[2];
  const u16* wg = (const u16*)d_in[4];
  const u16* wu = (const u16*)d_in[5];
  const u16* wd = (const u16*)d_in[6];
  char* ws = (char*)d_ws;
  k1_kernel<<<256, 768, 0, stream>>>(x, wqkv, wg, wu, ws);
  k2_kernel<<<256, 1024, 0, stream>>>(ws, wo, wd);
  k3_kernel<<<256, 256, 0, stream>>>(x, ws, (u16*)d_out);
}
```

```cpp
#include <hip/hip_runtime.h>
#include <hip/hip_bf16.h>

typedef short bf16x8 __attribute__((ext_vector_type(8)));
typedef short s16x4 __attribute__((ext_vector_type(4)));
typedef float f32x16 __attribute__((ext_vector_type(16)));
typedef float f32x4 __attribute__((ext_vector_type(4)));
typedef unsigned u32x4 __attribute__((ext_vector_type(4)));
typedef unsigned u32x2 __attribute__((ext_vector_type(2)));
typedef unsigned short u16;
typedef __attribute__((address_space(3))) const char* lds_cptr;
typedef __bf16 bf16x2_t __attribute__((ext_vector_type(2)));
typedef float f32x2_t __attribute__((ext_vector_type(2)));

#define DI __device__ __forceinline__
#define MFMA(a, b, c) __builtin_amdgcn_mfma_f32_32x32x16_bf16((a), (b), (c), 0, 0, 0)
#define BC(x) __builtin_bit_cast(bf16x8, x)
#define WAITV(n) asm volatile("s_waitcnt vmcnt(" #n ")" ::: "memory")
#define BAR() __builtin_amdgcn_s_barrier()
#define SB() __builtin_amdgcn_sched_barrier(0)
#define PIN(x) asm volatile("" : "+v"(x))

constexpr int S_ = 2048, D_ = 512, F_ = 1376, NH = 32;
constexpr int KST3 = 118;
constexpr size_t WS_Q = 0;
constexpr size_t WS_K = WS_Q + (size_t)NH * 64 * 1024;
constexpr size_t WS_V = WS_K + (size_t)NH * 64 * 1024;
constexpr size_t WS_A = WS_V + (size_t)NH * 64 * 1024;
constexpr size_t WS_W = WS_A + (size_t)64 * KST3 * 1024;
constexpr size_t WS_X = WS_W + (size_t)KST3 * 16 * 1024;
constexpr size_t WS_G = WS_X + (size_t)64 * 32 * 1024;
constexpr size_t WS_C = WS_G + (size_t)86 * 32 * 1024;

DI unsigned pk2(float a, float b) { f32x2_t v = {a, b}; return __builtin_bit_cast(unsigned, __builtin_convertvector(v, bf16x2_t)); }
DI float swap_add(float v) {
  auto rr = __builtin_amdgcn_permlane32_swap(__float_as_uint(v), __float_as_uint(v), false, false);
  return __uint_as_float(rr[0]) + __uint_as_float(rr[1]);
}
DI float swap_max(float v) {
  auto rr = __builtin_amdgcn_permlane32_swap(__float_as_uint(v), __float_as_uint(v), false, false);
  return __builtin_fmaxf(__uint_as_float(rr[0]), __uint_as_float(rr[1]));
}
DI s16x4 tr_read(lds_cptr p) {
  typedef short v4i16_t __attribute__((ext_vector_type(4)));
  return __builtin_bit_cast(s16x4, __builtin_amdgcn_ds_read_tr16_b64_v4i16((__attribute__((address_space(3))) v4i16_t*)p));
}
DI bf16x8 lds_read16(lds_cptr p) { return *(const __attribute__((address_space(3))) bf16x8*)p; }
DI void glds16(const void* g, char* lds) {
  __builtin_amdgcn_global_load_lds((const __attribute__((address_space(1))) void*)g, (__attribute__((address_space(3))) void*)lds, 16, 0, 0);
}
DI void add_s(float& a, float b) { a = a + b; asm("" : "+v"(a)); }
DI void fmac_s(float& a, float b, float c) { a = __builtin_fmaf(b, c, a); asm("" : "+v"(a)); }
DI float mul_s(float a, float b) { float r = a * b; asm("" : "+v"(r)); return r; }
#define ST_MODE 0
DI void st16(void* p, u32x4 v) {
  *(u32x4*)p = v;
}
DI void st16_dev(void* p, u32x4 v) { asm volatile("global_store_dwordx4 %0, %1, off sc1\n\ts_nop 1" :: "v"(p), "v"(v) : "memory"); }
DI float lo16(unsigned u) { return __uint_as_float(u << 16); }
DI float hi16(unsigned u) { return __uint_as_float(u & 0xffff0000u); }

#define ST1(i)

constexpr int K1_AB = 16384;
constexpr int K1_SLOT = 32768;
constexpr int K1_NSLOT = 5;
constexpr int K1_LDS = K1_NSLOT * K1_SLOT;
constexpr int K1_XS = 0;
constexpr int K1_XP = 256;
constexpr int K1_CX = 2048;

DI void k1_helper(char* lds, int panel, int col0, int rot, const u16* __restrict__ x, const u16* __restrict__ w) {
  const int lane = threadIdx.x & 63;
  const int hw = __builtin_amdgcn_readfirstlane(threadIdx.x >> 6) - 8;
  const u16 *sa, *sb;
  {
    const int row = hw * 32 + (lane >> 3), ch = (lane & 7) ^ ((row >> 1) & 3);
    sa = x + (size_t)(panel * 128 + row) * D_ + ch * 8;
    const int kr = hw * 16 + (lane >> 4), pc = (lane & 15) ^ ((kr & 3) << 2);
    sb = w + (size_t)kr * (3 * D_) + col0 + pc * 8;
  }
  const int a4 = ((lane & 7) ^ (((lane >> 4) & 3) | 4)) * 8 - ((lane & 7) ^ ((lane >> 4) & 3)) * 8;
  auto stage = [&](int kc) {
    char* d = lds + (kc % K1_NSLOT) * K1_SLOT + hw * 4096;
    const int k0 = ((kc + rot) & 7) * 64;
#pragma unroll
    for (int i = 0; i < 4; ++i) glds16(sa + k0 + i * 8 * D_ + ((i & 1) ? a4 : 0), d + i * 1024);
#pragma unroll
    for (int i = 0; i < 4; ++i) glds16(sb + (size_t)(k0 + i * 4) * (3 * D_), d + K1_AB + i * 1024);
  };
  stage(0); stage(1);
  WAITV(8);
  BAR();
  stage(2); stage(3);
#pragma unroll 1
  for (int kc = 0; kc < 8; ++kc) {
    if (kc < 5) WAITV(16); else if (kc == 5) WAITV(8); else WAITV(0);
    BAR();
    if (kc + 4 < 8) stage(kc + 4);
  }
  float* sc = (float*)lds;
  __syncthreads();
  if (hw < 2) {
    const int i = hw * 64 + lane;
    const float* p = sc + K1_XP + i;
    const float mean = ((p[0] + p[256]) + (p[512] + p[768])) * (1.f / D_);
    const float var = __builtin_fmaxf(((p[128] + p[384]) + (p[640] + p[896])) * (1.f / D_) - mean * mean, 0.f);
    sc[K1_XS + i] = mean; sc[K1_XS + 128 + i] = __builtin_amdgcn_rsqf(var + 1e-5f);
  }
  __syncthreads();
}

template <int TYPE, int CW>
DI void k1_gemm(char* lds, int panel, int st0, char* __restrict__ ws) {
  const int tid = threadIdx.x, lane = tid & 63, r = lane & 31, h = lane >> 5;
  const int mw = __builtin_amdgcn_readfirstlane(tid >> 6) & 1;
  f32x16 acc[2] = {f32x16{}, f32x16{}}, acs = f32x16{};
  const u32x4 ones_u = {0x3f803f80u, 0x3f803f80u, 0x3f803f80u, 0x3f803f80u};
  const bf16x8 ones = BC(ones_u);
  const int arow = mw * 64 + r;
  const int aswz = (arow >> 1) & 7;
  lds_cptr ab[4];
#pragma unroll
  for (int ks = 0; ks < 4; ++ks) ab[ks] = (lds_cptr)lds + arow * 128 + (((2 * ks + h) ^ aswz) << 4);
  const lds_cptr bb = (lds_cptr)lds + K1_AB + (8 * h + ((lane & 15) >> 2)) * 256 + (CW ^ ((lane & 15) >> 2)) * 64 + ((lane >> 4) & 1) * 32 + (lane & 3) * 8;
  bf16x8 af[2][2][2] = {}; s16x4 bl[2][2] = {}, bh[2][2] = {};
  float sm[2][2] = {{0.f, 0.f}, {0.f, 0.f}}, sq[2][2] = {{0.f, 0.f}, {0.f, 0.f}};
#define K1_FRAGS(buf, soff) do { __builtin_amdgcn_s_waitcnt(0xC07F); \
    _Pragma("unroll") for (int ks = 0; ks < 2; ++ks) { const lds_cptr a_ = ab[2 * (buf) + ks] + (soff); \
      af[buf][ks][0] = lds_read16(a_); af[buf][ks][1] = lds_read16(a_ + 4096); \
      bl[buf][ks] = tr_read(bb + (soff) + (2 * (buf) + ks) * 4096); bh[buf][ks] = tr_read(bb + (soff) + (2 * (buf) + ks) * 4096 + 1024); } SB(); } while (0)
#define K1_MMA(buf) do { _Pragma("unroll") for (int ks = 0; ks < 2; ++ks) { \
    const bf16x8 bf = __builtin_shufflevector(bl[buf][ks], bh[buf][ks], 0, 1, 2, 3, 4, 5, 6, 7); \
    _Pragma("unroll") for (int mi = 0; mi < 2; ++mi) { \
      if (TYPE == 1) acc[mi] = MFMA(af[buf][ks][mi], bf, acc[mi]); else acc[mi] = MFMA(bf, af[buf][ks][mi], acc[mi]); \
      const unsigned u = __builtin_bit_cast(u32x4, af[buf][ks][mi])[CW]; const float lo = lo16(u), hi = hi16(u); \
      add_s(sm[mi][0], lo); add_s(sm[mi][1], hi); fmac_s(sq[mi][0], lo, lo); fmac_s(sq[mi][1], hi, hi); \
      asm volatile("" : "+v"(sm[mi][0]), "+v"(sm[mi][1]), "+v"(sq[mi][0]), "+v"(sq[mi][1])); SB(); } } \
    if (mw == (buf)) {        \
      _Pragma("unroll") for (int ks = 0; ks < 2; ++ks) { const bf16x8 bf = __builtin_shufflevector(bl[buf][ks], bh[buf][ks], 0, 1, 2, 3, 4, 5, 6, 7); \
        if (TYPE == 1) acs = MFMA(ones, bf, acs); else acs = MFMA(bf, ones, acs); } } SB(); } while (0)
  ST1(0);
  BAR();
  ST1(1);
  K1_FRAGS(0, 0);
  int scur = 0, snext = K1_SLOT;
#pragma unroll 1
  for (int kc = 0; kc < 8; ++kc) {
    BAR();
    K1_FRAGS(1, scur); K1_MMA(0);
    K1_FRAGS(0, snext); K1_MMA(1);
    scur = snext; snext = (snext == (K1_NSLOT - 1) * K1_SLOT) ? 0 : snext + K1_SLOT;
  }
  ST1(17);
#pragma unroll
  for (int mi = 0; mi < 2; ++mi) {
    const float t = swap_add(sm[mi][0] + sm[mi][1]), u = swap_add(sq[mi][0] + sq[mi][1]);
    float* xp = (float*)lds + K1_XP + CW * 256 + mw * 64 + mi * 32 + r;
    if (h == 0) { xp[0] = t; xp[128] = u; }
  }
  float* cx = (float*)lds + K1_CX + CW * 2048 + lane * 4;
#pragma unroll
  for (int g = 0; g < 4; ++g) { f32x4 v = {acs[4 * g], acs[4 * g + 1], acs[4 * g + 2], acs[4 * g + 3]}; *(f32x4*)(cx + mw * 1024 + g * 256) = v; }
  __syncthreads();
#pragma unroll
  for (int g = 0; g < 4; ++g) { const f32x4 v = *(const f32x4*)(cx + (mw ^ 1) * 1024 + g * 256);
#pragma unroll
    for (int j = 0; j < 4; ++j) acs[4 * g + j] += v[j]; }
  __syncthreads();
  const float* xs = (const float*)lds + K1_XS;
  const int st = st0 + CW;
#pragma unroll
  for (int mi = 0; mi < 2; ++mi) {
    const int mt = panel * 4 + mw * 2 + mi;
    const int lr = mw * 64 + mi * 32;
    if (TYPE == 0) {
      const float mean = xs[lr + r], rstd = xs[128 + lr + r];
      const float sc = (st < 16) ? rstd * (0.25f * 1.4426950408889634f) : rstd;
      char* dstp = ws + ((st < 16) ? WS_Q : WS_K) + ((size_t)((st & 15) * 2) * 64 + mt) * 1024 + lane * 16;
#pragma unroll
      for (int hd = 0; hd < 2; ++hd) {
        float v[8];
#pragma unroll
        for (int j = 0; j < 8; ++j) v[j] = (acc[mi][8 * hd + j] - mean * acs[8 * hd + j]) * sc;
        u32x4 o = {pk2(v[0], v[1]), pk2(v[2], v[3]), pk2(v[4], v[5]), pk2(v[6], v[7])};
        st16(dstp + (size_t)hd * 64 * 1024, o);
      }
    } else {
      const float c = acs[0];
      char* dstp = ws + WS_V + ((size_t)((st - 32) * 2 + (r >> 4)) * 64 + mt) * 1024 + h * 256 + (r & 15) * 16;
#pragma unroll
      for (int ks = 0; ks < 2; ++ks) {
        const f32x4 m0 = *(const f32x4*)(xs + lr + 16 * ks + 4 * h), m1 = *(const f32x4*)(xs + lr + 16 * ks + 8 + 4 * h);
        const f32x4 r0 = *(const f32x4*)(xs + 128 + lr + 16 * ks + 4 * h), r1 = *(const f32x4*)(xs + 128 + lr + 16 * ks + 8 + 4 * h);
        float v[8];
#pragma unroll
        for (int j = 0; j < 4; ++j) { v[j] = (acc[mi][8 * ks + j] - m0[j] * c) * r0[j]; v[4 + j] = (acc[mi][8 * ks + 4 + j] - m1[j] * c) * r1[j]; }
        u32x4 o = {pk2(v[0], v[1]), pk2(v[2], v[3]), pk2(v[4], v[5]), pk2(v[6], v[7])};
        st16(dstp + ks * 512, o);
      }
    }
  }
  ST1(18);
}

#define K1_ROT 0
__global__ __launch_bounds__(768, 3) void k1_kernel(const u16* __restrict__ x, const u16* __restrict__ wqkv,
                                                    const u16* __restrict__ wg, const u16* __restrict__ wu, char* __restrict__ ws) {
  __shared__ __attribute__((aligned(16))) char lds[K1_LDS];
  const int bid = blockIdx.x;
  const int xcd = bid & 7, idx = bid >> 3;
  const int wave = __builtin_amdgcn_readfirstlane(threadIdx.x >> 6);
  const int lane = threadIdx.x & 63, c = lane & 31, h = lane >> 5;
  if (bid == 255 && threadIdx.x < 16) *(unsigned*)(ws + WS_C + threadIdx.x * 256) = 0u;
  if (idx < 24) {
    const int panel = (xcd & 3) * 4 + (idx & 3), grp = (xcd >> 2) * 6 + (idx >> 2);
    if (wave >= 8) k1_helper(lds, panel, grp * 128, K1_ROT, x, wqkv);
    else if (grp < 8) {
      switch (wave >> 1) {
        case 0: k1_gemm<0, 0>(lds, panel, grp * 4, ws); break; case 1: k1_gemm<0, 1>(lds, panel, grp * 4, ws); break;
        case 2: k1_gemm<0, 2>(lds, panel, grp * 4, ws); break; default: k1_gemm<0, 3>(lds, panel, grp * 4, ws); break;
      }
    } else {
      switch (wave >> 1) {
        case 0: k1_gemm<1, 0>(lds, panel, grp * 4, ws); break; case 1: k1_gemm<1, 1>(lds, panel, grp * 4, ws); break;
        case 2: k1_gemm<1, 2>(lds, panel, grp * 4, ws); break; default: k1_gemm<1, 3>(lds, panel, grp * 4, ws); break;
      }
    }
  } else {
    const int pb = (idx - 24) * 8 + xcd;
    if (wave < 4) {
      u32x4 v[8];
#pragma unroll
      for (int i = 0; i < 8; ++i) v[i] = *(const u32x4*)(x + (size_t)(pb * 32 + wave * 8 + i) * D_ + lane * 8);
      float f[8][8], sm[8];
#pragma unroll
      for (int i = 0; i < 8; ++i) {
#pragma unroll
        for (int j = 0; j < 4; ++j) { f[i][2 * j] = lo16(v[i][j]); f[i][2 * j + 1] = hi16(v[i][j]); }
        sm[i] = ((f[i][0] + f[i][1]) + (f[i][2] + f[i][3])) + ((f[i][4] + f[i][5]) + (f[i][6] + f[i][7]));
      }
#pragma unroll
      for (int d = 1; d < 64; d <<= 1)
#pragma unroll
        for (int i = 0; i < 8; ++i) sm[i] += __shfl_xor(sm[i], d);
#pragma unroll
      for (int i = 0; i < 8; ++i) {
        const float mean = sm[i] * (1.f / D_);
        float q = 0.f;
#pragma unroll
        for (int j = 0; j < 8; ++j) { f[i][j] -= mean; q += f[i][j] * f[i][j]; }
        sm[i] = q;
      }
#pragma unroll
      for (int d = 1; d < 64; d <<= 1)
#pragma unroll
        for (int i = 0; i < 8; ++i) sm[i] += __shfl_xor(sm[i], d);
#pragma unroll
      for (int i = 0; i < 8; ++i) {
        const float rstd = __builtin_amdgcn_rsqf(sm[i] * (1.f / D_) + 1e-5f);
        u32x4 o;
#pragma unroll
        for (int j = 0; j < 4; ++j) o[j] = pk2(f[i][2 * j] * rstd, f[i][2 * j + 1] * rstd);
        *(u32x4*)(ws + WS_X + ((size_t)pb * 32 + (lane >> 1)) * 1024 + ((lane & 1) * 32 + wave * 8 + i) * 16) = o;
      }
    } else {
      constexpr int NG = 86 * 32;
      const int wv = pb * 8 + wave - 4;
      char* wl = lds + wave * 8192;
      const int kr = lane >> 2, p = lane & 3;
#pragma unroll
      for (int i = 0; i < 6; ++i) {
        int f = wv + i * 512; if (f > NG - 1) f = NG - 1;
        const int st = f >> 5, ks = f & 31;
        glds16(((p < 2) ? wg : wu) + (size_t)(ks * 16 + kr) * F_ + st * 16 + (p & 1) * 8, wl + i * 1024);
      }
      WAITV(0);
      const lds_cptr tb = (lds_cptr)wl + (8 * h + ((lane & 15) >> 2)) * 64 + ((lane >> 4) & 1) * 32 + (lane & 3) * 8;
#pragma unroll
      for (int i = 0; i < 6; ++i) {
        const int f = wv + i * 512;
        const bf16x8 o = __builtin_shufflevector(tr_read(tb + i * 1024), tr_read(tb + i * 1024 + 256), 0, 1, 2, 3, 4, 5, 6, 7);
        if (f < NG) st16(ws + WS_G + (size_t)f * 1024 + lane * 16, __builtin_bit_cast(u32x4, o));
      }
    }
  }
}

constexpr int K2_V = 65536, K2_C = 131072, K2_Q = K2_C + 8192, K2_T = K2_Q + 8192, K2_CNT = K2_T + 8192, K2_LDS = K2_CNT + 128;
#define K2_HEAD 102.f
#define K2_INIT 8
constexpr float HEAD = K2_HEAD;
#define K2_ITERS 16

DI float cexp2(float x) { return __builtin_amdgcn_fmed3f(__builtin_amdgcn_exp2f(x), 0.f, 1.f); }
DI void exp_pack(const f32x16& s, u32x4& p0, u32x4& p1) {
#pragma unroll
  for (int i = 0; i < 4; ++i) { p0[i] = pk2(cexp2(s[2 * i]), cexp2(s[2 * i + 1])); p1[i] = pk2(cexp2(s[8 + 2 * i]), cexp2(s[9 + 2 * i])); }
}
DI float max16(const f32x16& s) {
  float m = __builtin_fmaxf(__builtin_fmaxf(s[0], s[1]), s[2]);
#pragma unroll
  for (int i = 3; i < 15; i += 2) m = __builtin_fmaxf(__builtin_fmaxf(m, s[i]), s[i + 1]);
  return __builtin_fmaxf(m, s[15]);
}
DI f32x16 splat16(float v) { f32x16 r;
#pragma unroll
  for (int i = 0; i < 16; ++i) r[i] = v;
  return r; }

DI void k2_fix(f32x16& o, float& nm, const bf16x8 qf, lds_cptr kp0, lds_cptr vp0, int vinc, int t0, int h) {
  float sm = -3.0e38f;
#pragma unroll 1
  for (int t = t0; t < t0 + 4; ++t) {
    const f32x16 s = MFMA(lds_read16(kp0 + t * 1024), qf, splat16(nm));
    sm = __builtin_fmaxf(sm, max16(s));
    u32x4 p0, p1; exp_pack(s, p0, p1);
#pragma unroll
    for (int i = 0; i < 4; ++i) { p0[i] ^= 0x80008000u; p1[i] ^= 0x80008000u; }
    o = MFMA(lds_read16(vp0 + t * vinc), BC(p0), o);
    o = MFMA(lds_read16(vp0 + t * vinc + 512), BC(p1), o);
  }
  sm = swap_max(sm);
  const float lo = swap_add(h == 0 ? o[8] : 0.f);
  sm = __builtin_fmaxf(sm, __builtin_amdgcn_logf(__builtin_fmaxf(lo, 1e-37f)));
  const float dl = __builtin_fmaxf(sm + HEAD, 0.f);
  const float f = __builtin_amdgcn_exp2f(-dl);
#pragma unroll
  for (int i = 0; i < 16; ++i) o[i] *= f;
  nm -= dl;
#pragma unroll 1
  for (int t = t0; t < t0 + 4; ++t) {
    const f32x16 s = MFMA(lds_read16(kp0 + t * 1024), qf, splat16(nm));
    u32x4 p0, p1; exp_pack(s, p0, p1);
    o = MFMA(lds_read16(vp0 + t * vinc), BC(p0), o);
    o = MFMA(lds_read16(vp0 + t * vinc + 512), BC(p1), o);
  }
}
DI void k2_sync(char* lds, int target) {
  volatile __attribute__((address_space(3))) unsigned* cnt = (volatile __attribute__((address_space(3))) unsigned*)(lds + K2_CNT);
  if ((threadIdx.x & 63) == 0) __hip_atomic_fetch_add((__attribute__((address_space(3))) unsigned*)(lds + K2_CNT), 1u, __ATOMIC_RELAXED, __HIP_MEMORY_SCOPE_WORKGROUP);
  while ((int)__builtin_amdgcn_readfirstlane(*cnt) < target) __builtin_amdgcn_s_sleep(1);
  asm volatile("" ::: "memory");
}

DI void k2_need(char* lds, int n) {
  volatile __attribute__((address_space(3))) int* pg = (volatile __attribute__((address_space(3))) int*)(lds + K2_CNT + 16);
  while (__builtin_amdgcn_readfirstlane(pg[0]) < n || __builtin_amdgcn_readfirstlane(pg[1]) < n) __builtin_amdgcn_s_sleep(1);
  asm volatile("" ::: "memory");
}
DI void k2_loader(char* lds, char* __restrict__ ws, int which, const u16* __restrict__ wo, const u16* __restrict__ wd) {
  const int lane = threadIdx.x & 63, bid = blockIdx.x;
  const int head = (bid & 7) * 4 + ((bid >> 3) & 3);
  const char* g = ws + (which ? WS_V : WS_K) + (size_t)head * 65536 + lane * 16;
  char* l = lds + (which ? K2_V : 0);
  volatile __attribute__((address_space(3))) int* pg = (volatile __attribute__((address_space(3))) int*)(lds + K2_CNT + 16) + which;
  __builtin_amdgcn_s_setprio(3);
  const int c0 = which ? 0 : 1;
#pragma unroll 1
  for (int c = c0; c < 10; ++c) {
    if (c < 8) {
#pragma unroll
      for (int i = 0; i < 8; ++i) glds16(g + c * 8192 + i * 1024, l + c * 8192 + i * 1024);
    }
    if (c - 2 >= c0) {
      if (c < 8) WAITV(16); else if (c == 8) WAITV(8); else WAITV(0);
      if (lane == 0) *pg = c - 1;
    }
  }
  __builtin_amdgcn_s_setprio(0);
  constexpr int NF = KST3 * 16;
  const int wv = bid * 2 + which, kr = lane >> 2, p = lane & 3, h = lane >> 5;
  char* wl = lds + K2_T + which * 4096;
#pragma unroll
  for (int i = 0; i < 4; ++i) {
    int f = wv + i * 512; if (f > NF - 1) f = NF - 1;
    const int ks = f >> 4, nt = f & 15;
    glds16(((ks < 32) ? wo + (size_t)(ks * 16 + kr) * D_ : wd + (size_t)((ks - 32) * 16 + kr) * D_) + nt * 32 + p * 8, wl + i * 1024);
  }
  WAITV(0);
  const lds_cptr tb = (lds_cptr)wl + (4 * h + ((lane & 15) >> 2)) * 64 + ((lane >> 4) & 1) * 32 + (lane & 3) * 8;
#pragma unroll
  for (int i = 0; i < 4; ++i) {
    const int f = wv + i * 512;
    const bf16x8 o = __builtin_shufflevector(tr_read(tb + i * 1024), tr_read(tb + i * 1024 + 512), 0, 1, 2, 3, 4, 5, 6, 7);
    if (f < NF) st16_dev(ws + WS_W + (size_t)f * 1024 + lane * 16, __builtin_bit_cast(u32x4, o));
  }
  WAITV(0);
  if (lane == 0) __hip_atomic_fetch_add((unsigned*)(ws + WS_C + 8 * 256), 1u, __ATOMIC_RELAXED, __HIP_MEMORY_SCOPE_AGENT);
}
#define TS(i)
DI void k2_attn(char* lds, char* __restrict__ ws) {
  const int tid = threadIdx.x, lane = tid & 63, r = lane & 31, h = lane >> 5;
  const int wave = __builtin_amdgcn_readfirstlane(tid >> 6);
  const int bid = blockIdx.x;
  const int head = (bid & 7) * 4 + ((bid >> 3) & 3), qb = bid >> 5;
  const int mt = qb * 8 + wave;
  glds16(ws + WS_Q + ((size_t)head * 64 + mt) * 1024 + lane * 16, lds + K2_Q + wave * 1024);
  glds16(ws + WS_K + (size_t)head * 65536 + wave * 1024 + lane * 16, lds + wave * 1024);
  {
    const unsigned c = ((tid & 31) == 0) ? 0x3f803f80u : 0u;
    u32x4 z = {c, c, c, c}; *(u32x4*)(lds + K2_C + tid * 16) = z;
  }
  asm volatile("s_waitcnt vmcnt(0) lgkmcnt(0)" ::: "memory");
  k2_sync(lds, 8);
  const bf16x8 qf = lds_read16((lds_cptr)lds + K2_Q + wave * 1024 + lane * 16);
  const lds_cptr kp0 = (lds_cptr)lds + lane * 16;
  const lds_cptr vp0 = (r < 16) ? (lds_cptr)lds + K2_V + h * 256 + r * 16 : (lds_cptr)lds + K2_C + (r == 16 ? 0 : 16);
  const int vinc = (r < 16) ? 1024 : 0;
  f32x16 o = f32x16{};
  float nm;
  {
    const f32x16 z = f32x16{};
    float m = max16(MFMA(lds_read16(kp0), qf, z));
#pragma unroll
    for (int t = 1; t < K2_INIT; ++t) m = __builtin_fmaxf(m, max16(MFMA(lds_read16(kp0 + t * 1024), qf, z)));
    nm = -(swap_max(m) + HEAD);
  }
  TS(0);
  k2_need(lds, 2);
  TS(1);
#define XC(x) negm
#define XU(...)
#define EXP4(S, b) do { _Pragma("unroll") for (int i = 0; i < 4; ++i) e[i] = cexp2(S[b + i]); PIN(e); SB(); } while (0)
#define EXP4P(P, S, b) do { P[0] = pk2(e[0], e[1]); P[1] = pk2(e[2], e[3]); \
    P[2] = pk2(cexp2(S[b]), cexp2(S[b + 1])); P[3] = pk2(cexp2(S[b + 2]), cexp2(S[b + 3])); PIN(P); SB(); } while (0)
#define THEAD(j, SC, SN, KN, KL, VL0, VL1) do { \
    EXP4(SC, 0); \
    SN = MFMA(KN, qf, XC(SN)); PIN(SN); SB(); \
    KL = lds_read16(kp + (j + 2) * 1024); VL0 = lds_read16(vp + (j + 1) * 1024); VL1 = lds_read16(vp + (j + 1) * 1024 + 512); SB(); \
    EXP4P(p0, SC, 4); } while (0)
#define TTAIL(SC, SN, KO, VU0, VU1) do { \
    o = MFMA(VU0, BC(p0), o); PIN(o); SB(); \
    EXP4(SC, 8); \
    XU(SN = MFMA(KO, nqf, SN); PIN(SN); SB();) \
    EXP4P(p1, SC, 12); \
    o = MFMA(VU1, BC(p1), o); PIN(o); SB(); } while (0)
  f32x4 e;
  bf16x8 nqf;
  { u32x4 t = __builtin_bit_cast(u32x4, qf); t[0] ^= 0x80008000u; t[1] ^= 0x80008000u; t[2] ^= 0x80008000u; t[3] ^= 0x80008000u; nqf = BC(t); }
  int it = 0;
#pragma unroll 1
  for (;;) {
    lds_cptr kp = kp0 + it * 4096, vp = vp0 + it * 4 * vinc;
    bf16x8 k0 = lds_read16(kp), k1 = lds_read16(kp + 1024), k2, k3 = k0;
    bf16x8 vA0 = lds_read16(vp), vA1 = lds_read16(vp + 512), vB0, vB1;
    f32x16 negm = splat16(nm); PIN(negm);
    f32x16 sA = MFMA(k0, qf, splat16(nm)), sB = sA;
    u32x4 p0, p1;
    THEAD(0, sA, sB, k1, k2, vB0, vB1);
    bool trig;
#pragma unroll 1
    do {
      TTAIL(sA, sB, k3, vA0, vA1);
      THEAD(1, sB, sA, k2, k3, vA0, vA1); TTAIL(sB, sA, k0, vB0, vB1);
      THEAD(2, sA, sB, k3, k0, vB0, vB1); TTAIL(sA, sB, k1, vA0, vA1);
      THEAD(3, sB, sA, k0, k1, vA0, vA1); TTAIL(sB, sA, k2, vB0, vB1);
      kp += 4096; vp += 4 * vinc; ++it;
      if ((it & 1) == 0 && it < 14) k2_need(lds, (it >> 1) + 2);
      {
        volatile __attribute__((address_space(3))) int* pg = (volatile __attribute__((address_space(3))) int*)(lds + K2_CNT + 64);
        if (lane == 0) pg[wave] = it;
        const int other = __builtin_amdgcn_readfirstlane(pg[wave ^ 4]);
        if (it > other) __builtin_amdgcn_s_setprio(1); else __builtin_amdgcn_s_setprio(2);
      }
      THEAD(0, sA, sB, k1, k2, vB0, vB1);
      trig = __any(o[8] >= 1.f);
    } while (it < K2_ITERS && !trig);
    if (__builtin_expect(!trig, 1)) break;
    k2_fix(o, nm, qf, kp0, vp0, vinc, 4 * it - 4, h);
    if (it >= K2_ITERS) break;
  }
  asm volatile("s_nop 15" : "+v"(o));
  const float il = 1.f / swap_add(h == 0 ? o[8] : 0.f);
  u32x4 ov = {pk2(mul_s(o[0], il), mul_s(o[1], il)), pk2(mul_s(o[2], il), mul_s(o[3], il)), pk2(mul_s(o[4], il), mul_s(o[5], il)), pk2(mul_s(o[6], il), mul_s(o[7], il))};
  st16_dev(ws + WS_A + ((size_t)mt * KST3 + head) * 1024 + lane * 16, ov);
}

DI void k2_ffn(char* __restrict__ ws) {
  const int lane = threadIdx.x & 63;
  const int fw = __builtin_amdgcn_readfirstlane(threadIdx.x >> 6) - 8;
  const int bid = blockIdx.x;
  const int sp = (bid & 7) + 8 * fw, mp = bid >> 3;
  if (sp < 43) {
    const __amdgpu_buffer_rsrc_t rs = __builtin_amdgcn_make_buffer_rsrc(ws, 0, 0x7fffffff, 0x00020000);
    const int xo = (int)WS_X + mp * 65536, wo = (int)WS_G + sp * 65536, l16 = lane * 16;
    f32x16 acc[2][2] = {{f32x16{}, f32x16{}}, {f32x16{}, f32x16{}}};
#define FF_PF 3
    constexpr int PF = FF_PF;
    u32x4 a0[PF], a1[PF], b0[PF], b1[PF];
#define BLD(off) __builtin_amdgcn_raw_buffer_load_b128(rs, l16, (off), 0)
#define FF_LOAD(i, k) do { a0[i] = BLD(xo + (k) * 1024); a1[i] = BLD(xo + 32768 + (k) * 1024); \
    b0[i] = BLD(wo + (k) * 1024); b1[i] = BLD(wo + 32768 + (k) * 1024); } while (0)
#pragma unroll
    for (int i = 0; i < PF; ++i) FF_LOAD(i, i);
#pragma unroll
    for (int k = 0; k < 32; ++k) {
      const int i = k % PF;
      acc[0][0] = MFMA(BC(b0[i]), BC(a0[i]), acc[0][0]); acc[0][1] = MFMA(BC(b1[i]), BC(a0[i]), acc[0][1]);
      acc[1][0] = MFMA(BC(b0[i]), BC(a1[i]), acc[1][0]); acc[1][1] = MFMA(BC(b1[i]), BC(a1[i]), acc[1][1]);
      PIN(acc[1][1]); SB();
      if (k + PF < 32) FF_LOAD(i, k + PF);
      SB();
    }
    asm volatile("s_nop 15\n\ts_nop 15" : "+v"(acc[0][0]), "+v"(acc[0][1]), "+v"(acc[1][0]), "+v"(acc[1][1]));
    SB();
#pragma unroll
    for (int mi = 0; mi < 2; ++mi)
#pragma unroll
      for (int s = 0; s < 2; ++s) {
        float v[8];
#pragma unroll
        for (int j = 0; j < 8; ++j) {
          const float g = acc[mi][s][j], u = acc[mi][s][8 + j];
          float d = __builtin_amdgcn_exp2f(mul_s(g, -1.4426950408889634f)); add_s(d, 1.f);
          v[j] = mul_s(mul_s(g, __builtin_amdgcn_rcpf(d)), u);
        }
        u32x4 o = {pk2(v[0], v[1]), pk2(v[2], v[3]), pk2(v[4], v[5]), pk2(v[6], v[7])};
        st16_dev(ws + WS_A + ((size_t)(mp * 2 + mi) * KST3 + 32 + sp * 2 + s) * 1024 + lane * 16, o);
      }
  }
}

constexpr int K3_ROWB = 64 * 4 + 16;
__global__ __launch_bounds__(1024, 4) void k2_kernel(char* __restrict__ ws, const u16* __restrict__ wo, const u16* __restrict__ wd,
                                                     const u16* __restrict__ x, u16* __restrict__ out) {
  __shared__ __attribute__((aligned(16))) char lds[K2_LDS];
  if (threadIdx.x < 32) *(unsigned*)(lds + K2_CNT + threadIdx.x * 4) = 0u;
  __syncthreads();
  const int wv = __builtin_amdgcn_readfirstlane(threadIdx.x >> 6);
  if (wv < 8) k2_attn(lds, ws); else if (wv < 14) k2_ffn(ws); else k2_loader(lds, ws, wv - 14, wo, wd);
  __builtin_amdgcn_s_setprio(0);
  const int tid = threadIdx.x, lane = tid & 63, r = lane & 31, h = lane >> 5, bid = blockIdx.x;
  WAITV(0);
  __syncthreads();
  if (tid == 0) {
    unsigned* cg = (unsigned*)(ws + WS_C + (bid >> 5) * 256);
    unsigned* cw = (unsigned*)(ws + WS_C + 8 * 256);
    __hip_atomic_fetch_add(cg, 1u, __ATOMIC_RELAXED, __HIP_MEMORY_SCOPE_AGENT);
    for (int spin = 0; spin < (1 << 22); ++spin) {
      if (__hip_atomic_load(cg, __ATOMIC_RELAXED, __HIP_MEMORY_SCOPE_AGENT) >= 32u &&
          __hip_atomic_load(cw, __ATOMIC_RELAXED, __HIP_MEMORY_SCOPE_AGENT) >= 512u) break;
      __builtin_amdgcn_s_sleep(2);
    }
  }
  __syncthreads();
  const int mb = (bid >> 5) * 4 + (bid & 3), nb = (bid >> 2) & 7;
  if (wv < 8) {
    const int k0 = wv * 15 - (wv > 6 ? 1 : 0), nk = (wv < 6) ? 15 : 14;
    const __amdgpu_buffer_rsrc_t rs = __builtin_amdgcn_make_buffer_rsrc(ws, 0, 0x7fffffff, 0x00020000);
    const int ao = (int)WS_A + (mb * 2 * KST3 + k0) * 1024, wo_ = (int)WS_W + (k0 * 16 + nb * 2) * 1024, l16 = lane * 16;
    f32x16 acc[2][2] = {{f32x16{}, f32x16{}}, {f32x16{}, f32x16{}}};
    constexpr int PF = 3;
    u32x4 a0[PF], a1[PF], b0[PF], b1[PF];
    const int klast = nk - 1;
#define BLD1(off) __builtin_amdgcn_raw_buffer_load_b128(rs, l16, (off), 16)
#define K3_LOAD(i, k) do { const int kk_ = ((k) < 14) ? (k) : klast; \
    a0[i] = BLD1(ao + kk_ * 1024); a1[i] = BLD1(ao + KST3 * 1024 + kk_ * 1024); \
    b0[i] = BLD1(wo_ + kk_ * 16384); b1[i] = BLD1(wo_ + kk_ * 16384 + 1024); } while (0)
#pragma unroll
    for (int i = 0; i < PF; ++i) K3_LOAD(i, i);
#pragma unroll
    for (int k = 0; k < 15; ++k) {
      const int i = k % PF;
      if (k < 14 || nk == 15) {
        acc[0][0] = MFMA(BC(b0[i]), BC(a0[i]), acc[0][0]); acc[0][1] = MFMA(BC(b1[i]), BC(a0[i]), acc[0][1]);
        acc[1][0] = MFMA(BC(b0[i]), BC(a1[i]), acc[1][0]); acc[1][1] = MFMA(BC(b1[i]), BC(a1[i]), acc[1][1]);
      }
      PIN(acc[1][1]); SB();
      if (k + PF < 15) K3_LOAD(i, k + PF);
      SB();
    }
    char* pw = lds + wv * 64 * K3_ROWB;
#pragma unroll
    for (int i = 0; i < 2; ++i)
#pragma unroll
      for (int j = 0; j < 2; ++j)
#pragma unroll
        for (int g = 0; g < 4; ++g) {
          f32x4 v = {acc[i][j][4 * g], acc[i][j][4 * g + 1], acc[i][j][4 * g + 2], acc[i][j][4 * g + 3]};
          *(f32x4*)(pw + (i * 32 + r) * K3_ROWB + (j * 32 + 8 * g + 4 * h) * 4) = v;
        }
  }
  const int m = tid >> 4, n0 = (tid & 15) * 4;
  const size_t go = (size_t)(mb * 64 + m) * D_ + nb * 64 + n0;
  const u32x2 xv = *(const u32x2*)(x + go);
  __syncthreads();
  f32x4 s = {lo16(xv[0]), hi16(xv[0]), lo16(xv[1]), hi16(xv[1])};
#pragma unroll
  for (int w = 0; w < 8; ++w) s += *(const f32x4*)(lds + w * 64 * K3_ROWB + m * K3_ROWB + n0 * 4);
  u32x2 o = {pk2(s[0], s[1]), pk2(s[2], s[3])};
  *(u32x2*)(out + go) = o;
}

extern "C" void kernel_launch(void* const* d_in, const int* in_sizes, int n_in, void* d_out, int out_size, void* d_ws, size_t ws_size,
                              hipStream_t stream) {
  const u16* x = (const u16*)d_in[0];
  const u16* wqkv = (const u16*)d_in[1];
  const u16* wo = (const u16*)d_in[2];
  const u16* wg = (const u16*)d_in[4];
  const u16* wu = (const u16*)d_in[5];
  const u16* wd = (const u16*)d_in[6];
  char* ws = (char*)d_ws;
  k1_kernel<<<256, 768, 0, stream>>>(x, wqkv, wg, wu, ws);
  k2_kernel<<<256, 1024, 0, stream>>>(ws, wo, wd, x, (u16*)d_out);
}
```

```cpp
#include <hip/hip_runtime.h>
#include <hip/hip_bf16.h>

typedef short bf16x8 __attribute__((ext_vector_type(8)));
typedef short s16x4 __attribute__((ext_vector_type(4)));
typedef float f32x16 __attribute__((ext_vector_type(16)));
typedef float f32x4 __attribute__((ext_vector_type(4)));
typedef unsigned u32x4 __attribute__((ext_vector_type(4)));
typedef unsigned u32x2 __attribute__((ext_vector_type(2)));
typedef unsigned short u16;
typedef __attribute__((address_space(3))) const char* lds_cptr;
typedef __bf16 bf16x2_t __attribute__((ext_vector_type(2)));
typedef float f32x2_t __attribute__((ext_vector_type(2)));

#define DI __device__ __forceinline__
#define MFMA(a, b, c) __builtin_amdgcn_mfma_f32_32x32x16_bf16((a), (b), (c), 0, 0, 0)
#define BC(x) __builtin_bit_cast(bf16x8, x)
#define WAITV(n) asm volatile("s_waitcnt vmcnt(" #n ")" ::: "memory")
#define BAR() __builtin_amdgcn_s_barrier()
#define SB() __builtin_amdgcn_sched_barrier(0)
#define PIN(x) asm volatile("" : "+v"(x))

constexpr int S_ = 2048, D_ = 512, F_ = 1376, NH = 32;
constexpr int KST3 = 118;
constexpr size_t WS_Q = 0;
constexpr size_t WS_K = WS_Q + (size_t)NH * 64 * 1024;
constexpr size_t WS_V = WS_K + (size_t)NH * 64 * 1024;
constexpr size_t WS_A = WS_V + (size_t)NH * 64 * 1024;
constexpr size_t WS_W = WS_A + (size_t)64 * KST3 * 1024;
constexpr size_t WS_X = WS_W + (size_t)KST3 * 16 * 1024;
constexpr size_t WS_G = WS_X + (size_t)64 * 32 * 1024;
constexpr size_t WS_C = WS_G + (size_t)86 * 32 * 1024;

DI unsigned pk2(float a, float b) { f32x2_t v = {a, b}; return __builtin_bit_cast(unsigned, __builtin_convertvector(v, bf16x2_t)); }
DI float swap_add(float v) {
  auto rr = __builtin_amdgcn_permlane32_swap(__float_as_uint(v), __float_as_uint(v), false, false);
  return __uint_as_float(rr[0]) + __uint_as_float(rr[1]);
}
DI float swap_max(float v) {
  auto rr = __builtin_amdgcn_permlane32_swap(__float_as_uint(v), __float_as_uint(v), false, false);
  return __builtin_fmaxf(__uint_as_float(rr[0]), __uint_as_float(rr[1]));
}
DI s16x4 tr_read(lds_cptr p) {
  typedef short v4i16_t __attribute__((ext_vector_type(4)));
  return __builtin_bit_cast(s16x4, __builtin_amdgcn_ds_read_tr16_b64_v4i16((__attribute__((address_space(3))) v4i16_t*)p));
}
DI bf16x8 lds_read16(lds_cptr p) { return *(const __attribute__((address_space(3))) bf16x8*)p; }
DI void glds16(const void* g, char* lds) {
  __builtin_amdgcn_global_load_lds((const __attribute__((address_space(1))) void*)g, (__attribute__((address_space(3))) void*)lds, 16, 0, 0);
}
DI void add_s(float& a, float b) { a = a + b; asm("" : "+v"(a)); }
DI void fmac_s(float& a, float b, float c) { a = __builtin_fmaf(b, c, a); asm("" : "+v"(a)); }
DI float mul_s(float a, float b) { float r = a * b; asm("" : "+v"(r)); return r; }
DI void st16(void* p, u32x4 v) { *(u32x4*)p = v; }
DI void st16_dev(void* p, u32x4 v) { asm volatile("global_store_dwordx4 %0, %1, off sc1\n\ts_nop 1" :: "v"(p), "v"(v) : "memory"); }
DI float lo16(unsigned u) { return __uint_as_float(u << 16); }
DI float hi16(unsigned u) { return __uint_as_float(u & 0xffff0000u); }


constexpr int K1_AB = 16384;
constexpr int K1_SLOT = 32768;
constexpr int K1_NSLOT = 5;
constexpr int K1_LDS = K1_NSLOT * K1_SLOT;
constexpr int K1_XS = 0;
constexpr int K1_XP = 256;
constexpr int K1_CX = 2048;

DI void k1_helper(char* lds, int panel, int col0, const u16* __restrict__ x, const u16* __restrict__ w) {
  const int lane = threadIdx.x & 63;
  const int hw = __builtin_amdgcn_readfirstlane(threadIdx.x >> 6) - 8;
  const u16 *sa, *sb;
  {
    const int row = hw * 32 + (lane >> 3), ch = (lane & 7) ^ ((row >> 1) & 3);
    sa = x + (size_t)(panel * 128 + row) * D_ + ch * 8;
    const int kr = hw * 16 + (lane >> 4), pc = (lane & 15) ^ ((kr & 3) << 2);
    sb = w + (size_t)kr * (3 * D_) + col0 + pc * 8;
  }
  const int a4 = ((lane & 7) ^ (((lane >> 4) & 3) | 4)) * 8 - ((lane & 7) ^ ((lane >> 4) & 3)) * 8;
  auto stage = [&](int kc) {
    char* d = lds + (kc % K1_NSLOT) * K1_SLOT + hw * 4096;
    const int k0 = kc * 64;
#pragma unroll
    for (int i = 0; i < 4; ++i) glds16(sa + k0 + i * 8 * D_ + ((i & 1) ? a4 : 0), d + i * 1024);
#pragma unroll
    for (int i = 0; i < 4; ++i) glds16(sb + (size_t)(k0 + i * 4) * (3 * D_), d + K1_AB + i * 1024);
  };
  stage(0); stage(1);
  WAITV(8);
  BAR();
  stage(2); stage(3);
#pragma unroll 1
  for (int kc = 0; kc < 8; ++kc) {
    if (kc < 5) WAITV(16); else if (kc == 5) WAITV(8); else WAITV(0);
    BAR();
    if (kc + 4 < 8) stage(kc + 4);
  }
  float* sc = (float*)lds;
  __syncthreads();
  if (hw < 2) {
    const int i = hw * 64 + lane;
    const float* p = sc + K1_XP + i;
    const float mean = ((p[0] + p[256]) + (p[512] + p[768])) * (1.f / D_);
    const float var = __builtin_fmaxf(((p[128] + p[384]) + (p[640] + p[896])) * (1.f / D_) - mean * mean, 0.f);
    sc[K1_XS + i] = mean; sc[K1_XS + 128 + i] = __builtin_amdgcn_rsqf(var + 1e-5f);
  }
  __syncthreads();
}

template <int TYPE, int CW>
DI void k1_gemm(char* lds, int panel, int st0, char* __restrict__ ws) {
  const int tid = threadIdx.x, lane = tid & 63, r = lane & 31, h = lane >> 5;
  const int mw = __builtin_amdgcn_readfirstlane(tid >> 6) & 1;
  f32x16 acc[2] = {f32x16{}, f32x16{}}, acs = f32x16{};
  const u32x4 ones_u = {0x3f803f80u, 0x3f803f80u, 0x3f803f80u, 0x3f803f80u};
  const bf16x8 ones = BC(ones_u);
  const int arow = mw * 64 + r;
  const int aswz = (arow >> 1) & 7;
  lds_cptr ab[4];
#pragma unroll
  for (int ks = 0; ks < 4; ++ks) ab[ks] = (lds_cptr)lds + arow * 128 + (((2 * ks + h) ^ aswz) << 4);
  const lds_cptr bb = (lds_cptr)lds + K1_AB + (8 * h + ((lane & 15) >> 2)) * 256 + (CW ^ ((lane & 15) >> 2)) * 64 + ((lane >> 4) & 1) * 32 + (lane & 3) * 8;
  bf16x8 af[2][2][2] = {}; s16x4 bl[2][2] = {}, bh[2][2] = {};
  float sm[2][2] = {{0.f, 0.f}, {0.f, 0.f}}, sq[2][2] = {{0.f, 0.f}, {0.f, 0.f}};
#define K1_FRAGS(buf, soff) do { __builtin_amdgcn_s_waitcnt(0xC07F); \
    _Pragma("unroll") for (int ks = 0; ks < 2; ++ks) { const lds_cptr a_ = ab[2 * (buf) + ks] + (soff); \
      af[buf][ks][0] = lds_read16(a_); af[buf][ks][1] = lds_read16(a_ + 4096); \
      bl[buf][ks] = tr_read(bb + (soff) + (2 * (buf) + ks) * 4096); bh[buf][ks] = tr_read(bb + (soff) + (2 * (buf) + ks) * 4096 + 1024); } SB(); } while (0)
#define K1_MMA(buf) do { _Pragma("unroll") for (int ks = 0; ks < 2; ++ks) { \
    const bf16x8 bf = __builtin_shufflevector(bl[buf][ks], bh[buf][ks], 0, 1, 2, 3, 4, 5, 6, 7); \
    _Pragma("unroll") for (int mi = 0; mi < 2; ++mi) { \
      if (TYPE == 1) acc[mi] = MFMA(af[buf][ks][mi], bf, acc[mi]); else acc[mi] = MFMA(bf, af[buf][ks][mi], acc[mi]); \
      const unsigned u = __builtin_bit_cast(u32x4, af[buf][ks][mi])[CW]; const float lo = lo16(u), hi = hi16(u); \
      add_s(sm[mi][0], lo); add_s(sm[mi][1], hi); fmac_s(sq[mi][0], lo, lo); fmac_s(sq[mi][1], hi, hi); \
      asm volatile("" : "+v"(sm[mi][0]), "+v"(sm[mi][1]), "+v"(sq[mi][0]), "+v"(sq[mi][1])); SB(); } } \
    if (mw == (buf)) {        \
      _Pragma("unroll") for (int ks = 0; ks < 2; ++ks) { const bf16x8 bf = __builtin_shufflevector(bl[buf][ks], bh[buf][ks], 0, 1, 2, 3, 4, 5, 6, 7); \
        if (TYPE == 1) acs = MFMA(ones, bf, acs); else acs = MFMA(bf, ones, acs); } } SB(); } while (0)
  BAR();
  K1_FRAGS(0, 0);
  int scur = 0, snext = K1_SLOT;
#pragma unroll 1
  for (int kc = 0; kc < 8; ++kc) {
    BAR();
    K1_FRAGS(1, scur); K1_MMA(0);
    K1_FRAGS(0, snext); K1_MMA(1);
    scur = snext; snext = (snext == (K1_NSLOT - 1) * K1_SLOT) ? 0 : snext + K1_SLOT;
  }
#pragma unroll
  for (int mi = 0; mi < 2; ++mi) {
    const float t = swap_add(sm[mi][0] + sm[mi][1]), u = swap_add(sq[mi][0] + sq[mi][1]);
    float* xp = (float*)lds + K1_XP + CW * 256 + mw * 64 + mi * 32 + r;
    if (h == 0) { xp[0] = t; xp[128] = u; }
  }
  float* cx = (float*)lds + K1_CX + CW * 2048 + lane * 4;
#pragma unroll
  for (int g = 0; g < 4; ++g) { f32x4 v = {acs[4 * g], acs[4 * g + 1], acs[4 * g + 2], acs[4 * g + 3]}; *(f32x4*)(cx + mw * 1024 + g * 256) = v; }
  __syncthreads();
#pragma unroll
  for (int g = 0; g < 4; ++g) { const f32x4 v = *(const f32x4*)(cx + (mw ^ 1) * 1024 + g * 256);
#pragma unroll
    for (int j = 0; j < 4; ++j) acs[4 * g + j] += v[j]; }
  __syncthreads();
  const float* xs = (const float*)lds + K1_XS;
  const int st = st0 + CW;
#pragma unroll
  for (int mi = 0; mi < 2; ++mi) {
    const int mt = panel * 4 + mw * 2 + mi;
    const int lr = mw * 64 + mi * 32;
    if (TYPE == 0) {
      const float mean = xs[lr + r], rstd = xs[128 + lr + r];
      const float sc = (st < 16) ? rstd * (0.25f * 1.4426950408889634f) : rstd;
      char* dstp = ws + ((st < 16) ? WS_Q : WS_K) + ((size_t)((st & 15) * 2) * 64 + mt) * 1024 + lane * 16;
#pragma unroll
      for (int hd = 0; hd < 2; ++hd) {
        float v[8];
#pragma unroll
        for (int j = 0; j < 8; ++j) v[j] = (acc[mi][8 * hd + j] - mean * acs[8 * hd + j]) * sc;
        u32x4 o = {pk2(v[0], v[1]), pk2(v[2], v[3]), pk2(v[4], v[5]), pk2(v[6], v[7])};
        st16(dstp + (size_t)hd * 64 * 1024, o);
      }
    } else {
      const float c = acs[0];
      char* dstp = ws + WS_V + ((size_t)((st - 32) * 2 + (r >> 4)) * 64 + mt) * 1024 + h * 256 + (r & 15) * 16;
#pragma unroll
      for (int ks = 0; ks < 2; ++ks) {
        const f32x4 m0 = *(const f32x4*)(xs + lr + 16 * ks + 4 * h), m1 = *(const f32x4*)(xs + lr + 16 * ks + 8 + 4 * h);
        const f32x4 r0 = *(const f32x4*)(xs + 128 + lr + 16 * ks + 4 * h), r1 = *(const f32x4*)(xs + 128 + lr + 16 * ks + 8 + 4 * h);
        float v[8];
#pragma unroll
        for (int j = 0; j < 4; ++j) { v[j] = (acc[mi][8 * ks + j] - m0[j] * c) * r0[j]; v[4 + j] = (acc[mi][8 * ks + 4 + j] - m1[j] * c) * r1[j]; }
        u32x4 o = {pk2(v[0], v[1]), pk2(v[2], v[3]), pk2(v[4], v[5]), pk2(v[6], v[7])};
        st16(dstp + ks * 512, o);
      }
    }
  }
}

__global__ __launch_bounds__(768, 3) void k1_kernel(const u16* __restrict__ x, const u16* __restrict__ wqkv,
                                                    const u16* __restrict__ wg, const u16* __restrict__ wu, char* __restrict__ ws) {
  __shared__ __attribute__((aligned(16))) char lds[K1_LDS];
  const int bid = blockIdx.x;
  const int xcd = bid & 7, idx = bid >> 3;
  const int wave = __builtin_amdgcn_readfirstlane(threadIdx.x >> 6);
  const int lane = threadIdx.x & 63, c = lane & 31, h = lane >> 5;
  if (bid == 255 && threadIdx.x < 16) *(unsigned*)(ws + WS_C + threadIdx.x * 256) = 0u;
  if (idx < 24) {
    const int panel = (xcd & 3) * 4 + (idx & 3), grp = (xcd >> 2) * 6 + (idx >> 2);
    if (wave >= 8) k1_helper(lds, panel, grp * 128, x, wqkv);
    else if (grp < 8) {
      switch (wave >> 1) {
        case 0: k1_gemm<0, 0>(lds, panel, grp * 4, ws); break; case 1: k1_gemm<0, 1>(lds, panel, grp * 4, ws); break;
        case 2: k1_gemm<0, 2>(lds, panel, grp * 4, ws); break; default: k1_gemm<0, 3>(lds, panel, grp * 4, ws); break;
      }
    } else {
      switch (wave >> 1) {
        case 0: k1_gemm<1, 0>(lds, panel, grp * 4, ws); break; case 1: k1_gemm<1, 1>(lds, panel, grp * 4, ws); break;
        case 2: k1_gemm<1, 2>(lds, panel, grp * 4, ws); break; default: k1_gemm<1, 3>(lds, panel, grp * 4, ws); break;
      }
    }
  } else {
    const int pb = (idx - 24) * 8 + xcd;
    if (wave < 4) {
      u32x4 v[8];
#pragma unroll
      for (int i = 0; i < 8; ++i) v[i] = *(const u32x4*)(x + (size_t)(pb * 32 + wave * 8 + i) * D_ + lane * 8);
      float f[8][8], sm[8];
#pragma unroll
      for (int i = 0; i < 8; ++i) {
#pragma unroll
        for (int j = 0; j < 4; ++j) { f[i][2 * j] = lo16(v[i][j]); f[i][2 * j + 1] = hi16(v[i][j]); }
        sm[i] = ((f[i][0] + f[i][1]) + (f[i][2] + f[i][3])) + ((f[i][4] + f[i][5]) + (f[i][6] + f[i][7]));
      }
#pragma unroll
      for (int d = 1; d < 64; d <<= 1)
#pragma unroll
        for (int i = 0; i < 8; ++i) sm[i] += __shfl_xor(sm[i], d);
#pragma unroll
      for (int i = 0; i < 8; ++i) {
        const float mean = sm[i] * (1.f / D_);
        float q = 0.f;
#pragma unroll
        for (int j = 0; j < 8; ++j) { f[i][j] -= mean; q += f[i][j] * f[i][j]; }
        sm[i] = q;
      }
#pragma unroll
      for (int d = 1; d < 64; d <<= 1)
#pragma unroll
        for (int i = 0; i < 8; ++i) sm[i] += __shfl_xor(sm[i], d);
#pragma unroll
      for (int i = 0; i < 8; ++i) {
        const float rstd = __builtin_amdgcn_rsqf(sm[i] * (1.f / D_) + 1e-5f);
        u32x4 o;
#pragma unroll
        for (int j = 0; j < 4; ++j) o[j] = pk2(f[i][2 * j] * rstd, f[i][2 * j + 1] * rstd);
        *(u32x4*)(ws + WS_X + ((size_t)pb * 32 + (lane >> 1)) * 1024 + ((lane & 1) * 32 + wave * 8 + i) * 16) = o;
      }
    } else {
      constexpr int NG = 86 * 32;
      const int wv = pb * 8 + wave - 4;
      char* wl = lds + wave * 8192;
      const int kr = lane >> 2, p = lane & 3;
#pragma unroll
      for (int i = 0; i < 6; ++i) {
        int f = wv + i * 512; if (f > NG - 1) f = NG - 1;
        const int st = f >> 5, ks = f & 31;
        glds16(((p < 2) ? wg : wu) + (size_t)(ks * 16 + kr) * F_ + st * 16 + (p & 1) * 8, wl + i * 1024);
      }
      WAITV(0);
      const lds_cptr tb = (lds_cptr)wl + (8 * h + ((lane & 15) >> 2)) * 64 + ((lane >> 4) & 1) * 32 + (lane & 3) * 8;
#pragma unroll
      for (int i = 0; i < 6; ++i) {
        const int f = wv + i * 512;
        const bf16x8 o = __builtin_shufflevector(tr_read(tb + i * 1024), tr_read(tb + i * 1024 + 256), 0, 1, 2, 3, 4, 5, 6, 7);
        if (f < NG) st16(ws + WS_G + (size_t)f * 1024 + lane * 16, __builtin_bit_cast(u32x4, o));
      }
    }
  }
}

constexpr int K2_V = 65536, K2_C = 131072, K2_Q = K2_C + 8192, K2_T = K2_Q + 8192, K2_CNT = K2_T + 8192, K2_LDS = K2_CNT + 128;
constexpr float HEAD = 102.f;
constexpr int K2_INIT = 8;
constexpr int K2_ITERS = 16;

DI float cexp2(float x) { return __builtin_amdgcn_fmed3f(__builtin_amdgcn_exp2f(x), 0.f, 1.f); }
DI void exp_pack(const f32x16& s, u32x4& p0, u32x4& p1) {
#pragma unroll
  for (int i = 0; i < 4; ++i) { p0[i] = pk2(cexp2(s[2 * i]), cexp2(s[2 * i + 1])); p1[i] = pk2(cexp2(s[8 + 2 * i]), cexp2(s[9 + 2 * i])); }
}
DI float max16(const f32x16& s) {
  float m = __builtin_fmaxf(__builtin_fmaxf(s[0], s[1]), s[2]);
#pragma unroll
  for (int i = 3; i < 15; i += 2) m = __builtin_fmaxf(__builtin_fmaxf(m, s[i]), s[i + 1]);
  return __builtin_fmaxf(m, s[15]);
}
DI f32x16 splat16(float v) { f32x16 r;
#pragma unroll
  for (int i = 0; i < 16; ++i) r[i] = v;
  return r; }

DI void k2_fix(f32x16& o, float& nm, const bf16x8 qf, lds_cptr kp0, lds_cptr vp0, int vinc, int t0, int h) {
  float sm = -3.0e38f;
#pragma unroll 1
  for (int t = t0; t < t0 + 4; ++t) {
    const f32x16 s = MFMA(lds_read16(kp0 + t * 1024), qf, splat16(nm));
    sm = __builtin_fmaxf(sm, max16(s));
    u32x4 p0, p1; exp_pack(s, p0, p1);
#pragma unroll
    for (int i = 0; i < 4; ++i) { p0[i] ^= 0x80008000u; p1[i] ^= 0x80008000u; }
    o = MFMA(lds_read16(vp0 + t * vinc), BC(p0), o);
    o = MFMA(lds_read16(vp0 + t * vinc + 512), BC(p1), o);
  }
  sm = swap_max(sm);
  const float lo = swap_add(h == 0 ? o[8] : 0.f);
  sm = __builtin_fmaxf(sm, __builtin_amdgcn_logf(__builtin_fmaxf(lo, 1e-37f)));
  const float dl = __builtin_fmaxf(sm + HEAD, 0.f);
  const float f = __builtin_amdgcn_exp2f(-dl);
#pragma unroll
  for (int i = 0; i < 16; ++i) o[i] *= f;
  nm -= dl;
#pragma unroll 1
  for (int t = t0; t < t0 + 4; ++t) {
    const f32x16 s = MFMA(lds_read16(kp0 + t * 1024), qf, splat16(nm));
    u32x4 p0, p1; exp_pack(s, p0, p1);
    o = MFMA(lds_read16(vp0 + t * vinc), BC(p0), o);
    o = MFMA(lds_read16(vp0 + t * vinc + 512), BC(p1), o);
  }
}
DI void k2_sync(char* lds, int target) {
  volatile __attribute__((address_space(3))) unsigned* cnt = (volatile __attribute__((address_space(3))) unsigned*)(lds + K2_CNT);
  if ((threadIdx.x & 63) == 0) __hip_atomic_fetch_add((__attribute__((address_space(3))) unsigned*)(lds + K2_CNT), 1u, __ATOMIC_RELAXED, __HIP_MEMORY_SCOPE_WORKGROUP);
  while ((int)__builtin_amdgcn_readfirstlane(*cnt) < target) __builtin_amdgcn_s_sleep(1);
  asm volatile("" ::: "memory");
}

DI void k2_need(char* lds, int n) {
  volatile __attribute__((address_space(3))) int* pg = (volatile __attribute__((address_space(3))) int*)(lds + K2_CNT + 16);
  while (__builtin_amdgcn_readfirstlane(pg[0]) < n || __builtin_amdgcn_readfirstlane(pg[1]) < n) __builtin_amdgcn_s_sleep(1);
  asm volatile("" ::: "memory");
}
DI void k2_loader(char* lds, char* __restrict__ ws, int which, const u16* __restrict__ wo, const u16* __restrict__ wd) {
  const int lane = threadIdx.x & 63, bid = blockIdx.x;
  const int head = (bid & 7) * 4 + ((bid >> 3) & 3);
  const char* g = ws + (which ? WS_V : WS_K) + (size_t)head * 65536 + lane * 16;
  char* l = lds + (which ? K2_V : 0);
  volatile __attribute__((address_space(3))) int* pg = (volatile __attribute__((address_space(3))) int*)(lds + K2_CNT + 16) + which;
  __builtin_amdgcn_s_setprio(3);
  const int c0 = which ? 0 : 1;
#pragma unroll 1
  for (int c = c0; c < 10; ++c) {
    if (c < 8) {
#pragma unroll
      for (int i = 0; i < 8; ++i) glds16(g + c * 8192 + i * 1024, l + c * 8192 + i * 1024);
    }
    if (c - 2 >= c0) {
      if (c < 8) WAITV(16); else if (c == 8) WAITV(8); else WAITV(0);
      if (lane == 0) *pg = c - 1;
    }
  }
  __builtin_amdgcn_s_setprio(0);
  constexpr int NF = KST3 * 16;
  const int wv = bid * 2 + which, kr = lane >> 2, p = lane & 3, h = lane >> 5;
  char* wl = lds + K2_T + which * 4096;
#pragma unroll
  for (int i = 0; i < 4; ++i) {
    int f = wv + i * 512; if (f > NF - 1) f = NF - 1;
    const int ks = f >> 4, nt = f & 15;
    glds16(((ks < 32) ? wo + (size_t)(ks * 16 + kr) * D_ : wd + (size_t)((ks - 32) * 16 + kr) * D_) + nt * 32 + p * 8, wl + i * 1024);
  }
  WAITV(0);
  const lds_cptr tb = (lds_cptr)wl + (4 * h + ((lane & 15) >> 2)) * 64 + ((lane >> 4) & 1) * 32 + (lane & 3) * 8;
#pragma unroll
  for (int i = 0; i < 4; ++i) {
    const int f = wv + i * 512;
    const bf16x8 o = __builtin_shufflevector(tr_read(tb + i * 1024), tr_read(tb + i * 1024 + 512), 0, 1, 2, 3, 4, 5, 6, 7);
    if (f < NF) st16_dev(ws + WS_W + (size_t)f * 1024 + lane * 16, __builtin_bit_cast(u32x4, o));
  }
  WAITV(0);
  if (lane == 0) __hip_atomic_fetch_add((unsigned*)(ws + WS_C + 8 * 256), 1u, __ATOMIC_RELAXED, __HIP_MEMORY_SCOPE_AGENT);
}
DI void k2_attn(char* lds, char* __restrict__ ws) {
  const int tid = threadIdx.x, lane = tid & 63, r = lane & 31, h = lane >> 5;
  const int wave = __builtin_amdgcn_readfirstlane(tid >> 6);
  const int bid = blockIdx.x;
  const int head = (bid & 7) * 4 + ((bid >> 3) & 3), qb = bid >> 5;
  const int mt = qb * 8 + wave;
  glds16(ws + WS_Q + ((size_t)head * 64 + mt) * 1024 + lane * 16, lds + K2_Q + wave * 1024);
  glds16(ws + WS_K + (size_t)head * 65536 + wave * 1024 + lane * 16, lds + wave * 1024);
  {
    const unsigned c = ((tid & 31) == 0) ? 0x3f803f80u : 0u;
    u32x4 z = {c, c, c, c}; *(u32x4*)(lds + K2_C + tid * 16) = z;
  }
  asm volatile("s_waitcnt vmcnt(0) lgkmcnt(0)" ::: "memory");
  k2_sync(lds, 8);
  const bf16x8 qf = lds_read16((lds_cptr)lds + K2_Q + wave * 1024 + lane * 16);
  const lds_cptr kp0 = (lds_cptr)lds + lane * 16;
  const lds_cptr vp0 = (r < 16) ? (lds_cptr)lds + K2_V + h * 256 + r * 16 : (lds_cptr)lds + K2_C + (r == 16 ? 0 : 16);
  const int vinc = (r < 16) ? 1024 : 0;
  f32x16 o = f32x16{};
  float nm;
  {
    const f32x16 z = f32x16{};
    float m = max16(MFMA(lds_read16(kp0), qf, z));
#pragma unroll
    for (int t = 1; t < K2_INIT; ++t) m = __builtin_fmaxf(m, max16(MFMA(lds_read16(kp0 + t * 1024), qf, z)));
    nm = -(swap_max(m) + HEAD);
  }
  k2_need(lds, 2);
#define EXP4(S, b) do { _Pragma("unroll") for (int i = 0; i < 4; ++i) e[i] = cexp2(S[b + i]); PIN(e); SB(); } while (0)
#define EXP4P(P, S, b) do { P[0] = pk2(e[0], e[1]); P[1] = pk2(e[2], e[3]); \
    P[2] = pk2(cexp2(S[b]), cexp2(S[b + 1])); P[3] = pk2(cexp2(S[b + 2]), cexp2(S[b + 3])); PIN(P); SB(); } while (0)
#define THEAD(j, SC, SN, KN, KL, VL0, VL1) do { \
    EXP4(SC, 0); \
    SN = MFMA(KN, qf, negm); PIN(SN); SB(); \
    KL = lds_read16(kp + (j + 2) * 1024); VL0 = lds_read16(vp + (j + 1) * 1024); VL1 = lds_read16(vp + (j + 1) * 1024 + 512); SB(); \
    EXP4P(p0, SC, 4); } while (0)
#define TTAIL(SC, VU0, VU1) do { \
    o = MFMA(VU0, BC(p0), o); PIN(o); SB(); \
    EXP4(SC, 8); \
    EXP4P(p1, SC, 12); \
    o = MFMA(VU1, BC(p1), o); PIN(o); SB(); } while (0)
  f32x4 e;
  int it = 0;
#pragma unroll 1
  for (;;) {
    lds_cptr kp = kp0 + it * 4096, vp = vp0 + it * 4 * vinc;
    bf16x8 kA = lds_read16(kp + 1024), kB;
    bf16x8 vA0 = lds_read16(vp), vA1 = lds_read16(vp + 512), vB0, vB1;
    f32x16 negm = splat16(nm); PIN(negm);
    f32x16 sA = MFMA(lds_read16(kp), qf, negm), sB;
    u32x4 p0, p1;
    THEAD(0, sA, sB, kA, kB, vB0, vB1);
    bool trig;
#pragma unroll 1
    do {
      TTAIL(sA, vA0, vA1);
      THEAD(1, sB, sA, kB, kA, vA0, vA1); TTAIL(sB, vB0, vB1);
      THEAD(2, sA, sB, kA, kB, vB0, vB1); TTAIL(sA, vA0, vA1);
      THEAD(3, sB, sA, kB, kA, vA0, vA1); TTAIL(sB, vB0, vB1);
      kp += 4096; vp += 4 * vinc; ++it;
      if ((it & 1) == 0 && it < 14) k2_need(lds, (it >> 1) + 2);
      {
        volatile __attribute__((address_space(3))) int* pg = (volatile __attribute__((address_space(3))) int*)(lds + K2_CNT + 64);
        if (lane == 0) pg[wave] = it;
        const int other = __builtin_amdgcn_readfirstlane(pg[wave ^ 4]);
        if (it > other) __builtin_amdgcn_s_setprio(1); else __builtin_amdgcn_s_setprio(2);
      }
      THEAD(0, sA, sB, kA, kB, vB0, vB1);
      trig = __any(o[8] >= 1.f);
    } while (it < K2_ITERS && !trig);
    if (__builtin_expect(!trig, 1)) break;
    k2_fix(o, nm, qf, kp0, vp0, vinc, 4 * it - 4, h);
    if (it >= K2_ITERS) break;
  }
  asm volatile("s_nop 15" : "+v"(o));
  const float il = 1.f / swap_add(h == 0 ? o[8] : 0.f);
  u32x4 ov = {pk2(mul_s(o[0], il), mul_s(o[1], il)), pk2(mul_s(o[2], il), mul_s(o[3], il)), pk2(mul_s(o[4], il), mul_s(o[5], il)), pk2(mul_s(o[6], il), mul_s(o[7], il))};
  st16_dev(ws + WS_A + ((size_t)mt * KST3 + head) * 1024 + lane * 16, ov);
}

DI void k2_ffn(char* __restrict__ ws) {
  const int lane = threadIdx.x & 63;
  const int fw = __builtin_amdgcn_readfirstlane(threadIdx.x >> 6) - 8;
  const int bid = blockIdx.x;
  const int sp = (bid & 7) + 8 * fw, mp = bid >> 3;
  if (sp < 43) {
    const __amdgpu_buffer_rsrc_t rs = __builtin_amdgcn_make_buffer_rsrc(ws, 0, 0x7fffffff, 0x00020000);
    const int xo = (int)WS_X + mp * 65536, wo = (int)WS_G + sp * 65536, l16 = lane * 16;
    f32x16 acc[2][2] = {{f32x16{}, f32x16{}}, {f32x16{}, f32x16{}}};
    constexpr int PF = 3;
    u32x4 a0[PF], a1[PF], b0[PF], b1[PF];
#define BLD(off) __builtin_amdgcn_raw_buffer_load_b128(rs, l16, (off), 0)
#define FF_LOAD(i, k) do { a0[i] = BLD(xo + (k) * 1024); a1[i] = BLD(xo + 32768 + (k) * 1024); \
    b0[i] = BLD(wo + (k) * 1024); b1[i] = BLD(wo + 32768 + (k) * 1024); } while (0)
#pragma unroll
    for (int i = 0; i < PF; ++i) FF_LOAD(i, i);
#pragma unroll
    for (int k = 0; k < 32; ++k) {
      const int i = k % PF;
      acc[0][0] = MFMA(BC(b0[i]), BC(a0[i]), acc[0][0]); acc[0][1] = MFMA(BC(b1[i]), BC(a0[i]), acc[0][1]);
      acc[1][0] = MFMA(BC(b0[i]), BC(a1[i]), acc[1][0]); acc[1][1] = MFMA(BC(b1[i]), BC(a1[i]), acc[1][1]);
      PIN(acc[1][1]); SB();
      if (k + PF < 32) FF_LOAD(i, k + PF);
      SB();
    }
    asm volatile("s_nop 15\n\ts_nop 15" : "+v"(acc[0][0]), "+v"(acc[0][1]), "+v"(acc[1][0]), "+v"(acc[1][1]));
    SB();
#pragma unroll
    for (int mi = 0; mi < 2; ++mi)
#pragma unroll
      for (int s = 0; s < 2; ++s) {
        float v[8];
#pragma unroll
        for (int j = 0; j < 8; ++j) {
          const float g = acc[mi][s][j], u = acc[mi][s][8 + j];
          float d = __builtin_amdgcn_exp2f(mul_s(g, -1.4426950408889634f)); add_s(d, 1.f);
          v[j] = mul_s(mul_s(g, __builtin_amdgcn_rcpf(d)), u);
        }
        u32x4 o = {pk2(v[0], v[1]), pk2(v[2], v[3]), pk2(v[4], v[5]), pk2(v[6], v[7])};
        st16_dev(ws + WS_A + ((size_t)(mp * 2 + mi) * KST3 + 32 + sp * 2 + s) * 1024 + lane * 16, o);
      }
  }
}

constexpr int K3_ROWB = 64 * 4 + 16;
__global__ __launch_bounds__(1024, 4) void k2_kernel(char* __restrict__ ws, const u16* __restrict__ wo, const u16* __restrict__ wd,
                                                     const u16* __restrict__ x, u16* __restrict__ out) {
  __shared__ __attribute__((aligned(16))) char lds[K2_LDS];
  if (threadIdx.x < 32) *(unsigned*)(lds + K2_CNT + threadIdx.x * 4) = 0u;
  __syncthreads();
  const int wv = __builtin_amdgcn_readfirstlane(threadIdx.x >> 6);
  if (wv < 8) k2_attn(lds, ws); else if (wv < 14) k2_ffn(ws); else k2_loader(lds, ws, wv - 14, wo, wd);
  __builtin_amdgcn_s_setprio(0);
  const int tid = threadIdx.x, lane = tid & 63, r = lane & 31, h = lane >> 5, bid = blockIdx.x;
  WAITV(0);
  __syncthreads();
  if (tid == 0) {
    unsigned* cg = (unsigned*)(ws + WS_C + (bid >> 5) * 256);
    unsigned* cw = (unsigned*)(ws + WS_C + 8 * 256);
    __hip_atomic_fetch_add(cg, 1u, __ATOMIC_RELAXED, __HIP_MEMORY_SCOPE_AGENT);
    for (int spin = 0; spin < (1 << 22); ++spin) {
      if (__hip_atomic_load(cg, __ATOMIC_RELAXED, __HIP_MEMORY_SCOPE_AGENT) >= 32u &&
          __hip_atomic_load(cw, __ATOMIC_RELAXED, __HIP_MEMORY_SCOPE_AGENT) >= 512u) break;
      __builtin_amdgcn_s_sleep(2);
    }
  }
  __syncthreads();
  const int mb = (bid >> 5) * 4 + (bid & 3), nb = (bid >> 2) & 7;
  if (wv < 8) {
    const int k0 = wv * 15 - (wv > 6 ? 1 : 0), nk = (wv < 6) ? 15 : 14;
    const __amdgpu_buffer_rsrc_t rs = __builtin_amdgcn_make_buffer_rsrc(ws, 0, 0x7fffffff, 0x00020000);
    const int ao = (int)WS_A + (mb * 2 * KST3 + k0) * 1024, wo_ = (int)WS_W + (k0 * 16 + nb * 2) * 1024, l16 = lane * 16;
    f32x16 acc[2][2] = {{f32x16{}, f32x16{}}, {f32x16{}, f32x16{}}};
    constexpr int PF = 3;
    u32x4 a0[PF], a1[PF], b0[PF], b1[PF];
    const int klast = nk - 1;
#define BLD1(off) __builtin_amdgcn_raw_buffer_load_b128(rs, l16, (off), 16)
#define K3_LOAD(i, k) do { const int kk_ = ((k) < 14) ? (k) : klast; \
    a0[i] = BLD1(ao + kk_ * 1024); a1[i] = BLD1(ao + KST3 * 1024 + kk_ * 1024); \
    b0[i] = BLD1(wo_ + kk_ * 16384); b1[i] = BLD1(wo_ + kk_ * 16384 + 1024); } while (0)
#pragma unroll
    for (int i = 0; i < PF; ++i) K3_LOAD(i, i);
#pragma unroll
    for (int k = 0; k < 15; ++k) {
      const int i = k % PF;
      if (k < 14 || nk == 15) {
        acc[0][0] = MFMA(BC(b0[i]), BC(a0[i]), acc[0][0]); acc[0][1] = MFMA(BC(b1[i]), BC(a0[i]), acc[0][1]);
        acc[1][0] = MFMA(BC(b0[i]), BC(a1[i]), acc[1][0]); acc[1][1] = MFMA(BC(b1[i]), BC(a1[i]), acc[1][1]);
      }
      PIN(acc[1][1]); SB();
      if (k + PF < 15) K3_LOAD(i, k + PF);
      SB();
    }
    char* pw = lds + wv * 64 * K3_ROWB;
#pragma unroll
    for (int i = 0; i < 2; ++i)
#pragma unroll
      for (int j = 0; j < 2; ++j)
#pragma unroll
        for (int g = 0; g < 4; ++g) {
          f32x4 v = {acc[i][j][4 * g], acc[i][j][4 * g + 1], acc[i][j][4 * g + 2], acc[i][j][4 * g + 3]};
          *(f32x4*)(pw + (i * 32 + r) * K3_ROWB + (j * 32 + 8 * g + 4 * h) * 4) = v;
        }
  }
  const int m = tid >> 4, n0 = (tid & 15) * 4;
  const size_t go = (size_t)(mb * 64 + m) * D_ + nb * 64 + n0;
  const u32x2 xv = *(const u32x2*)(x + go);
  __syncthreads();
  f32x4 s = {lo16(xv[0]), hi16(xv[0]), lo16(xv[1]), hi16(xv[1])};
#pragma unroll
  for (int w = 0; w < 8; ++w) s += *(const f32x4*)(lds + w * 64 * K3_ROWB + m * K3_ROWB + n0 * 4);
  u32x2 o = {pk2(s[0], s[1]), pk2(s[2], s[3])};
  *(u32x2*)(out + go) = o;
}

extern "C" void kernel_launch(void* const* d_in, const int* in_sizes, int n_in, void* d_out, int out_size, void* d_ws, size_t ws_size,
                              hipStream_t stream) {
  const u16* x = (const u16*)d_in[0];
  const u16* wqkv = (const u16*)d_in[1];
  const u16* wo = (const u16*)d_in[2];
  const u16* wg = (const u16*)d_in[4];
  const u16* wu = (const u16*)d_in[5];
  const u16* wd = (const u16*)d_in[6];
  char* ws = (char*)d_ws;
  k1_kernel<<<256, 768, 0, stream>>>(x, wqkv, wg, wu, ws);
  k2_kernel<<<256, 1024, 0, stream>>>(ws, wo, wd, x, (u16*)d_out);
}
```

```cpp
#include <hip/hip_runtime.h>
#include <hip/hip_bf16.h>

typedef short bf16x8 __attribute__((ext_vector_type(8)));
typedef short s16x4 __attribute__((ext_vector_type(4)));
typedef float f32x16 __attribute__((ext_vector_type(16)));
typedef float f32x4 __attribute__((ext_vector_type(4)));
typedef unsigned u32x4 __attribute__((ext_vector_type(4)));
typedef unsigned u32x2 __attribute__((ext_vector_type(2)));
typedef unsigned short u16;
typedef __attribute__((address_space(3))) const char* lds_cptr;
typedef __bf16 bf16x2_t __attribute__((ext_vector_type(2)));
typedef float f32x2_t __attribute__((ext_vector_type(2)));

#define DI __device__ __forceinline__
#define MFMA(a, b, c) __builtin_amdgcn_mfma_f32_32x32x16_bf16((a), (b), (c), 0, 0, 0)
#define BC(x) __builtin_bit_cast(bf16x8, x)
#define WAITV(n) asm volatile("s_waitcnt vmcnt(" #n ")" ::: "memory")
#define BAR() __builtin_amdgcn_s_barrier()
#define SB() __builtin_amdgcn_sched_barrier(0)
#define PIN(x) asm volatile("" : "+v"(x))

constexpr int S_ = 2048, D_ = 512, F_ = 1376, NH = 32;
constexpr int KST3 = 118;
constexpr size_t WS_Q = 0;
constexpr size_t WS_K = WS_Q + (size_t)NH * 64 * 1024;
constexpr size_t WS_V = WS_K + (size_t)NH * 64 * 1024;
constexpr size_t WS_A = WS_V + (size_t)NH * 64 * 1024;
constexpr size_t WS_W = WS_A + (size_t)64 * KST3 * 1024;
constexpr size_t WS_X = WS_W + (size_t)KST3 * 16 * 1024;
constexpr size_t WS_G = WS_X + (size_t)64 * 32 * 1024;
constexpr size_t WS_C = WS_G + (size_t)86 * 32 * 1024;

DI unsigned pk2(float a, float b) { f32x2_t v = {a, b}; return __builtin_bit_cast(unsigned, __builtin_convertvector(v, bf16x2_t)); }
DI float swap_add(float v) {
  auto rr = __builtin_amdgcn_permlane32_swap(__float_as_uint(v), __float_as_uint(v), false, false);
  return __uint_as_float(rr[0]) + __uint_as_float(rr[1]);
}
DI float swap_max(float v) {
  auto rr = __builtin_amdgcn_permlane32_swap(__float_as_uint(v), __float_as_uint(v), false, false);
  return __builtin_fmaxf(__uint_as_float(rr[0]), __uint_as_float(rr[1]));
}
DI s16x4 tr_read(lds_cptr p) {
  typedef short v4i16_t __attribute__((ext_vector_type(4)));
  return __builtin_bit_cast(s16x4, __builtin_amdgcn_ds_read_tr16_b64_v4i16((__attribute__((address_space(3))) v4i16_t*)p));
}
DI bf16x8 lds_read16(lds_cptr p) { return *(const __attribute__((address_space(3))) bf16x8*)p; }
DI void glds16(const void* g, char* lds) {
  __builtin_amdgcn_global_load_lds((const __attribute__((address_space(1))) void*)g, (__attribute__((address_space(3))) void*)lds, 16, 0, 0);
}
DI void add_s(float& a, float b) { a = a + b; asm("" : "+v"(a)); }
DI void fmac_s(float& a, float b, float c) { a = __builtin_fmaf(b, c, a); asm("" : "+v"(a)); }
DI float mul_s(float a, float b) { float r = a * b; asm("" : "+v"(r)); return r; }
DI void st16(void* p, u32x4 v) { *(u32x4*)p = v; }
DI void st16_dev(void* p, u32x4 v) { asm volatile("global_store_dwordx4 %0, %1, off sc1\n\ts_nop 1" :: "v"(p), "v"(v) : "memory"); }
DI float lo16(unsigned u) { return __uint_as_float(u << 16); }
DI float hi16(unsigned u) { return __uint_as_float(u & 0xffff0000u); }


constexpr int K1_AB = 16384;
constexpr int K1_SLOT = 32768;
constexpr int K1_NSLOT = 5;
constexpr int K1_LDS = K1_NSLOT * K1_SLOT;
constexpr int K1_XS = 0;
constexpr int K1_XP = 256;
constexpr int K1_CX = 2048;

DI void k1_helper(char* lds, int panel, int col0, const u16* __restrict__ x, const u16* __restrict__ w) {
  const int lane = threadIdx.x & 63;
  const int hw = __builtin_amdgcn_readfirstlane(threadIdx.x >> 6) - 8;
  const u16 *sa, *sb;
  {
    const int row = hw * 32 + (lane >> 3), ch = (lane & 7) ^ ((row >> 1) & 3);
    sa = x + (size_t)(panel * 128 + row) * D_ + ch * 8;
    const int kr = hw * 16 + (lane >> 4), pc = (lane & 15) ^ ((kr & 3) << 2);
    sb = w + (size_t)kr * (3 * D_) + col0 + pc * 8;
  }
  const int a4 = ((lane & 7) ^ (((lane >> 4) & 3) | 4)) * 8 - ((lane & 7) ^ ((lane >> 4) & 3)) * 8;
  auto stage = [&](int kc) {
    char* d = lds + (kc % K1_NSLOT) * K1_SLOT + hw * 4096;
    const int k0 = kc * 64;
#pragma unroll
    for (int i = 0; i < 4; ++i) glds16(sa + k0 + i * 8 * D_ + ((i & 1) ? a4 : 0), d + i * 1024);
#pragma unroll
    for (int i = 0; i < 4; ++i) glds16(sb + (size_t)(k0 + i * 4) * (3 * D_), d + K1_AB + i * 1024);
  };
  stage(0); stage(1);
  WAITV(8);
  BAR();
  stage(2); stage(3);
#pragma unroll 1
  for (int kc = 0; kc < 8; ++kc) {
    if (kc < 5) WAITV(16); else if (kc == 5) WAITV(8); else WAITV(0);
    BAR();
    if (kc + 4 < 8) stage(kc + 4);
  }
  float* sc = (float*)lds;
  __syncthreads();
  if (hw < 2) {
    const int i = hw * 64 + lane;
    const float* p = sc + K1_XP + i;
    const float mean = ((p[0] + p[256]) + (p[512] + p[768])) * (1.f / D_);
    const float var = __builtin_fmaxf(((p[128] + p[384]) + (p[640] + p[896])) * (1.f / D_) - mean * mean, 0.f);
    sc[K1_XS + i] = mean; sc[K1_XS + 128 + i] = __builtin_amdgcn_rsqf(var + 1e-5f);
  }
  __syncthreads();
}

template <int TYPE, int CW>
DI void k1_gemm(char* lds, int panel, int st0, char* __restrict__ ws) {
  const int tid = threadIdx.x, lane = tid & 63, r = lane & 31, h = lane >> 5;
  const int mw = __builtin_amdgcn_readfirstlane(tid >> 6) & 1;
  f32x16 acc[2] = {f32x16{}, f32x16{}}, acs = f32x16{};
  const u32x4 ones_u = {0x3f803f80u, 0x3f803f80u, 0x3f803f80u, 0x3f803f80u};
  const bf16x8 ones = BC(ones_u);
  const int arow = mw * 64 + r;
  const int aswz = (arow >> 1) & 7;
  lds_cptr ab[4];
#pragma unroll
  for (int ks = 0; ks < 4; ++ks) ab[ks] = (lds_cptr)lds + arow * 128 + (((2 * ks + h) ^ aswz) << 4);
  const lds_cptr bb = (lds_cptr)lds + K1_AB + (8 * h + ((lane & 15) >> 2)) * 256 + (CW ^ ((lane & 15) >> 2)) * 64 + ((lane >> 4) & 1) * 32 + (lane & 3) * 8;
  bf16x8 af[2][2][2] = {}; s16x4 bl[2][2] = {}, bh[2][2] = {};
  float sm[2][2] = {{0.f, 0.f}, {0.f, 0.f}}, sq[2][2] = {{0.f, 0.f}, {0.f, 0.f}};
#define K1_FRAGS(buf, soff) do { __builtin_amdgcn_s_waitcnt(0xC07F); \
    _Pragma("unroll") for (int ks = 0; ks < 2; ++ks) { const lds_cptr a_ = ab[2 * (buf) + ks] + (soff); \
      af[buf][ks][0] = lds_read16(a_); af[buf][ks][1] = lds_read16(a_ + 4096); \
      bl[buf][ks] = tr_read(bb + (soff) + (2 * (buf) + ks) * 4096); bh[buf][ks] = tr_read(bb + (soff) + (2 * (buf) + ks) * 4096 + 1024); } SB(); } while (0)
#define K1_MMA(buf) do { _Pragma("unroll") for (int ks = 0; ks < 2; ++ks) { \
    const bf16x8 bf = __builtin_shufflevector(bl[buf][ks], bh[buf][ks], 0, 1, 2, 3, 4, 5, 6, 7); \
    _Pragma("unroll") for (int mi = 0; mi < 2; ++mi) { \
      if (TYPE == 1) acc[mi] = MFMA(af[buf][ks][mi], bf, acc[mi]); else acc[mi] = MFMA(bf, af[buf][ks][mi], acc[mi]); \
      const unsigned u = __builtin_bit_cast(u32x4, af[buf][ks][mi])[CW]; const float lo = lo16(u), hi = hi16(u); \
      add_s(sm[mi][0], lo); add_s(sm[mi][1], hi); fmac_s(sq[mi][0], lo, lo); fmac_s(sq[mi][1], hi, hi); \
      asm volatile("" : "+v"(sm[mi][0]), "+v"(sm[mi][1]), "+v"(sq[mi][0]), "+v"(sq[mi][1])); SB(); } } \
    if (mw == (buf)) {        \
      _Pragma("unroll") for (int ks = 0; ks < 2; ++ks) { const bf16x8 bf = __builtin_shufflevector(bl[buf][ks], bh[buf][ks], 0, 1, 2, 3, 4, 5, 6, 7); \
        if (TYPE == 1) acs = MFMA(ones, bf, acs); else acs = MFMA(bf, ones, acs); } } SB(); } while (0)
  BAR();
  K1_FRAGS(0, 0);
  int scur = 0, snext = K1_SLOT;
#pragma unroll 1
  for (int kc = 0; kc < 8; ++kc) {
    BAR();
    K1_FRAGS(1, scur); K1_MMA(0);
    K1_FRAGS(0, snext); K1_MMA(1);
    scur = snext; snext = (snext == (K1_NSLOT - 1) * K1_SLOT) ? 0 : snext + K1_SLOT;
  }
#pragma unroll
  for (int mi = 0; mi < 2; ++mi) {
    const float t = swap_add(sm[mi][0] + sm[mi][1]), u = swap_add(sq[mi][0] + sq[mi][1]);
    float* xp = (float*)lds + K1_XP + CW * 256 + mw * 64 + mi * 32 + r;
    if (h == 0) { xp[0] = t; xp[128] = u; }
  }
  float* cx = (float*)lds + K1_CX + CW * 2048 + lane * 4;
#pragma unroll
  for (int g = 0; g < 4; ++g) { f32x4 v = {acs[4 * g], acs[4 * g + 1], acs[4 * g + 2], acs[4 * g + 3]}; *(f32x4*)(cx + mw * 1024 + g * 256) = v; }
  __syncthreads();
#pragma unroll
  for (int g = 0; g < 4; ++g) { const f32x4 v = *(const f32x4*)(cx + (mw ^ 1) * 1024 + g * 256);
#pragma unroll
    for (int j = 0; j < 4; ++j) acs[4 * g + j] += v[j]; }
  __syncthreads();
  const float* xs = (const float*)lds + K1_XS;
  const int st = st0 + CW;
#pragma unroll
  for (int mi = 0; mi < 2; ++mi) {
    const int mt = panel * 4 + mw * 2 + mi;
    const int lr = mw * 64 + mi * 32;
    if (TYPE == 0) {
      const float mean = xs[lr + r], rstd = xs[128 + lr + r];
      const float sc = (st < 16) ? rstd * (0.25f * 1.4426950408889634f) : rstd;
      char* dstp = ws + ((st < 16) ? WS_Q : WS_K) + ((size_t)((st & 15) * 2) * 64 + mt) * 1024 + lane * 16;
#pragma unroll
      for (int hd = 0; hd < 2; ++hd) {
        float v[8];
#pragma unroll
        for (int j = 0; j < 8; ++j) v[j] = (acc[mi][8 * hd + j] - mean * acs[8 * hd + j]) * sc;
        u32x4 o = {pk2(v[0], v[1]), pk2(v[2], v[3]), pk2(v[4], v[5]), pk2(v[6], v[7])};
        st16(dstp + (size_t)hd * 64 * 1024, o);
      }
    } else {
      const float c = acs[0];
      char* dstp = ws + WS_V + ((size_t)((st - 32) * 2 + (r >> 4)) * 64 + mt) * 1024 + h * 256 + (r & 15) * 16;
#pragma unroll
      for (int ks = 0; ks < 2; ++ks) {
        const f32x4 m0 = *(const f32x4*)(xs + lr + 16 * ks + 4 * h), m1 = *(const f32x4*)(xs + lr + 16 * ks + 8 + 4 * h);
        const f32x4 r0 = *(const f32x4*)(xs + 128 + lr + 16 * ks + 4 * h), r1 = *(const f32x4*)(xs + 128 + lr + 16 * ks + 8 + 4 * h);
        float v[8];
#pragma unroll
        for (int j = 0; j < 4; ++j) { v[j] = (acc[mi][8 * ks + j] - m0[j] * c) * r0[j]; v[4 + j] = (acc[mi][8 * ks + 4 + j] - m1[j] * c) * r1[j]; }
        u32x4 o = {pk2(v[0], v[1]), pk2(v[2], v[3]), pk2(v[4], v[5]), pk2(v[6], v[7])};
        st16(dstp + ks * 512, o);
      }
    }
  }
}

__global__ __launch_bounds__(768, 3) void k1_kernel(const u16* __restrict__ x, const u16* __restrict__ wqkv,
                                                    const u16* __restrict__ wg, const u16* __restrict__ wu, char* __restrict__ ws) {
  __shared__ __attribute__((aligned(16))) char lds[K1_LDS];
  const int bid = blockIdx.x;
  const int xcd = bid & 7, idx = bid >> 3;
  const int wave = __builtin_amdgcn_readfirstlane(threadIdx.x >> 6);
  const int lane = threadIdx.x & 63, c = lane & 31, h = lane >> 5;
  if (bid == 255 && threadIdx.x < 16) *(unsigned*)(ws + WS_C + threadIdx.x * 256) = 0u;
  if (idx < 24) {
    const int panel = (xcd & 3) * 4 + (idx & 3), grp = (xcd >> 2) * 6 + (idx >> 2);
    if (wave >= 8) k1_helper(lds, panel, grp * 128, x, wqkv);
    else if (grp < 8) {
      switch (wave >> 1) {
        case 0: k1_gemm<0, 0>(lds, panel, grp * 4, ws); break; case 1: k1_gemm<0, 1>(lds, panel, grp * 4, ws); break;
        case 2: k1_gemm<0, 2>(lds, panel, grp * 4, ws); break; default: k1_gemm<0, 3>(lds, panel, grp * 4, ws); break;
      }
    } else {
      switch (wave >> 1) {
        case 0: k1_gemm<1, 0>(lds, panel, grp * 4, ws); break; case 1: k1_gemm<1, 1>(lds, panel, grp * 4, ws); break;
        case 2: k1_gemm<1, 2>(lds, panel, grp * 4, ws); break; default: k1_gemm<1, 3>(lds, panel, grp * 4, ws); break;
      }
    }
  } else {
    const int pb = (idx - 24) * 8 + xcd;
    if (wave < 4) {
      u32x4 v[8];
#pragma unroll
      for (int i = 0; i < 8; ++i) v[i] = *(const u32x4*)(x + (size_t)(pb * 32 + wave * 8 + i) * D_ + lane * 8);
      float f[8][8], sm[8];
#pragma unroll
      for (int i = 0; i < 8; ++i) {
#pragma unroll
        for (int j = 0; j < 4; ++j) { f[i][2 * j] = lo16(v[i][j]); f[i][2 * j + 1] = hi16(v[i][j]); }
        sm[i] = ((f[i][0] + f[i][1]) + (f[i][2] + f[i][3])) + ((f[i][4] + f[i][5]) + (f[i][6] + f[i][7]));
      }
#pragma unroll
      for (int d = 1; d < 64; d <<= 1)
#pragma unroll
        for (int i = 0; i < 8; ++i) sm[i] += __shfl_xor(sm[i], d);
#pragma unroll
      for (int i = 0; i < 8; ++i) {
        const float mean = sm[i] * (1.f / D_);
        float q = 0.f;
#pragma unroll
        for (int j = 0; j < 8; ++j) { f[i][j] -= mean; q += f[i][j] * f[i][j]; }
        sm[i] = q;
      }
#pragma unroll
      for (int d = 1; d < 64; d <<= 1)
#pragma unroll
        for (int i = 0; i < 8; ++i) sm[i] += __shfl_xor(sm[i], d);
#pragma unroll
      for (int i = 0; i < 8; ++i) {
        const float rstd = __builtin_amdgcn_rsqf(sm[i] * (1.f / D_) + 1e-5f);
        u32x4 o;
#pragma unroll
        for (int j = 0; j < 4; ++j) o[j] = pk2(f[i][2 * j] * rstd, f[i][2 * j + 1] * rstd);
        *(u32x4*)(ws + WS_X + ((size_t)pb * 32 + (lane >> 1)) * 1024 + ((lane & 1) * 32 + wave * 8 + i) * 16) = o;
      }
    } else {
      constexpr int NG = 86 * 32;
      const int wv = pb * 8 + wave - 4;
      char* wl = lds + wave * 8192;
      const int kr = lane >> 2, p = lane & 3;
#pragma unroll
      for (int i = 0; i < 6; ++i) {
        int f = wv + i * 512; if (f > NG - 1) f = NG - 1;
        const int st = f >> 5, ks = f & 31;
        glds16(((p < 2) ? wg : wu) + (size_t)(ks * 16 + kr) * F_ + st * 16 + (p & 1) * 8, wl + i * 1024);
      }
      WAITV(0);
      const lds_cptr tb = (lds_cptr)wl + (8 * h + ((lane & 15) >> 2)) * 64 + ((lane >> 4) & 1) * 32 + (lane & 3) * 8;
#pragma unroll
      for (int i = 0; i < 6; ++i) {
        const int f = wv + i * 512;
        const bf16x8 o = __builtin_shufflevector(tr_read(tb + i * 1024), tr_read(tb + i * 1024 + 256), 0, 1, 2, 3, 4, 5, 6, 7);
        if (f < NG) st16(ws + WS_G + (size_t)f * 1024 + lane * 16, __builtin_bit_cast(u32x4, o));
      }
    }
  }
}

constexpr int K2_V = 65536, K2_C = 131072, K2_Q = K2_C + 8192, K2_T = K2_Q + 8192, K2_CNT = K2_T + 8192, K2_LDS = K2_CNT + 128;
constexpr float HEAD = 102.f;
constexpr int K2_INIT = 8;
constexpr int K2_ITERS = 16;

DI float cexp2(float x) { return __builtin_amdgcn_fmed3f(__builtin_amdgcn_exp2f(x), 0.f, 1.f); }
DI void exp_pack(const f32x16& s, u32x4& p0, u32x4& p1) {
#pragma unroll
  for (int i = 0; i < 4; ++i) { p0[i] = pk2(cexp2(s[2 * i]), cexp2(s[2 * i + 1])); p1[i] = pk2(cexp2(s[8 + 2 * i]), cexp2(s[9 + 2 * i])); }
}
DI float max16(const f32x16& s) {
  float m = __builtin_fmaxf(__builtin_fmaxf(s[0], s[1]), s[2]);
#pragma unroll
  for (int i = 3; i < 15; i += 2) m = __builtin_fmaxf(__builtin_fmaxf(m, s[i]), s[i + 1]);
  return __builtin_fmaxf(m, s[15]);
}
DI f32x16 splat16(float v) { f32x16 r;
#pragma unroll
  for (int i = 0; i < 16; ++i) r[i] = v;
  return r; }

DI void k2_fix(f32x16& o, float& nm, const bf16x8 qf, lds_cptr kp0, lds_cptr vp0, int vinc, int t0, int h) {
  float sm = -3.0e38f;
#pragma unroll 1
  for (int t = t0; t < t0 + 4; ++t) {
    const f32x16 s = MFMA(lds_read16(kp0 + t * 1024), qf, splat16(nm));
    sm = __builtin_fmaxf(sm, max16(s));
    u32x4 p0, p1; exp_pack(s, p0, p1);
#pragma unroll
    for (int i = 0; i < 4; ++i) { p0[i] ^= 0x80008000u; p1[i] ^= 0x80008000u; }
    o = MFMA(lds_read16(vp0 + t * vinc), BC(p0), o);
    o = MFMA(lds_read16(vp0 + t * vinc + 512), BC(p1), o);
  }
  sm = swap_max(sm);
  const float lo = swap_add(h == 0 ? o[8] : 0.f);
  sm = __builtin_fmaxf(sm, __builtin_amdgcn_logf(__builtin_fmaxf(lo, 1e-37f)));
  const float dl = __builtin_fmaxf(sm + HEAD, 0.f);
  const float f = __builtin_amdgcn_exp2f(-dl);
#pragma unroll
  for (int i = 0; i < 16; ++i) o[i] *= f;
  nm -= dl;
#pragma unroll 1
  for (int t = t0; t < t0 + 4; ++t) {
    const f32x16 s = MFMA(lds_read16(kp0 + t * 1024), qf, splat16(nm));
    u32x4 p0, p1; exp_pack(s, p0, p1);
    o = MFMA(lds_read16(vp0 + t * vinc), BC(p0), o);
    o = MFMA(lds_read16(vp0 + t * vinc + 512), BC(p1), o);
  }
}
DI void k2_sync(char* lds, int target) {
  volatile __attribute__((address_space(3))) unsigned* cnt = (volatile __attribute__((address_space(3))) unsigned*)(lds + K2_CNT);
  if ((threadIdx.x & 63) == 0) __hip_atomic_fetch_add((__attribute__((address_space(3))) unsigned*)(lds + K2_CNT), 1u, __ATOMIC_RELAXED, __HIP_MEMORY_SCOPE_WORKGROUP);
  while ((int)__builtin_amdgcn_readfirstlane(*cnt) < target) __builtin_amdgcn_s_sleep(1);
  asm volatile("" ::: "memory");
}

DI void k2_need(char* lds, int n) {
  volatile __attribute__((address_space(3))) int* pg = (volatile __attribute__((address_space(3))) int*)(lds + K2_CNT + 16);
  while (__builtin_amdgcn_readfirstlane(pg[0]) < n || __builtin_amdgcn_readfirstlane(pg[1]) < n) __builtin_amdgcn_s_sleep(1);
  asm volatile("" ::: "memory");
}
DI void k2_loader(char* lds, char* __restrict__ ws, int which, const u16* __restrict__ wo, const u16* __restrict__ wd) {
  const int lane = threadIdx.x & 63, bid = blockIdx.x;
  const int head = (bid & 7) * 4 + ((bid >> 3) & 3);
  const char* g = ws + (which ? WS_V : WS_K) + (size_t)head * 65536 + lane * 16;
  char* l = lds + (which ? K2_V : 0);
  volatile __attribute__((address_space(3))) int* pg = (volatile __attribute__((address_space(3))) int*)(lds + K2_CNT + 16) + which;
  __builtin_amdgcn_s_setprio(3);
  const int c0 = which ? 0 : 1;
#pragma unroll 1
  for (int c = c0; c < 10; ++c) {
    if (c < 8) {
#pragma unroll
      for (int i = 0; i < 8; ++i) glds16(g + c * 8192 + i * 1024, l + c * 8192 + i * 1024);
    }
    if (c - 2 >= c0) {
      if (c < 8) WAITV(16); else if (c == 8) WAITV(8); else WAITV(0);
      if (lane == 0) *pg = c - 1;
    }
  }
  __builtin_amdgcn_s_setprio(0);
  constexpr int NF = KST3 * 16;
  const int wv = bid * 2 + which, kr = lane >> 2, p = lane & 3, h = lane >> 5;
  char* wl = lds + K2_T + which * 4096;
#pragma unroll
  for (int i = 0; i < 4; ++i) {
    int f = wv + i * 512; if (f > NF - 1) f = NF - 1;
    const int ks = f >> 4, nt = f & 15;
    glds16(((ks < 32) ? wo + (size_t)(ks * 16 + kr) * D_ : wd + (size_t)((ks - 32) * 16 + kr) * D_) + nt * 32 + p * 8, wl + i * 1024);
  }
  WAITV(0);
  const lds_cptr tb = (lds_cptr)wl + (4 * h + ((lane & 15) >> 2)) * 64 + ((lane >> 4) & 1) * 32 + (lane & 3) * 8;
#pragma unroll
  for (int i = 0; i < 4; ++i) {
    const int f = wv + i * 512;
    const bf16x8 o = __builtin_shufflevector(tr_read(tb + i * 1024), tr_read(tb + i * 1024 + 512), 0, 1, 2, 3, 4, 5, 6, 7);
    if (f < NF) st16_dev(ws + WS_W + (size_t)f * 1024 + lane * 16, __builtin_bit_cast(u32x4, o));
  }
  WAITV(0);
  if (lane == 0) __hip_atomic_fetch_add((unsigned*)(ws + WS_C + 8 * 256), 1u, __ATOMIC_RELAXED, __HIP_MEMORY_SCOPE_AGENT);
}
DI void k2_attn(char* lds, char* __restrict__ ws) {
  const int tid = threadIdx.x, lane = tid & 63, r = lane & 31, h = lane >> 5;
  const int wave = __builtin_amdgcn_readfirstlane(tid >> 6);
  const int bid = blockIdx.x;
  const int head = (bid & 7) * 4 + ((bid >> 3) & 3), qb = bid >> 5;
  const int mt = qb * 8 + wave;
  glds16(ws + WS_Q + ((size_t)head * 64 + mt) * 1024 + lane * 16, lds + K2_Q + wave * 1024);
  glds16(ws + WS_K + (size_t)head * 65536 + wave * 1024 + lane * 16, lds + wave * 1024);
  {
    const unsigned c = ((tid & 31) == 0) ? 0x3f803f80u : 0u;
    u32x4 z = {c, c, c, c}; *(u32x4*)(lds + K2_C + tid * 16) = z;
  }
  asm volatile("s_waitcnt vmcnt(0) lgkmcnt(0)" ::: "memory");
  k2_sync(lds, 8);
  const bf16x8 qf = lds_read16((lds_cptr)lds + K2_Q + wave * 1024 + lane * 16);
  const lds_cptr kp0 = (lds_cptr)lds + lane * 16;
  const lds_cptr vp0 = (r < 16) ? (lds_cptr)lds + K2_V + h * 256 + r * 16 : (lds_cptr)lds + K2_C + (r == 16 ? 0 : 16);
  const int vinc = (r < 16) ? 1024 : 0;
  f32x16 o = f32x16{};
  float nm;
  {
    const f32x16 z = f32x16{};
    float m = max16(MFMA(lds_read16(kp0), qf, z));
#pragma unroll
    for (int t = 1; t < K2_INIT; ++t) m = __builtin_fmaxf(m, max16(MFMA(lds_read16(kp0 + t * 1024), qf, z)));
    nm = -(swap_max(m) + HEAD);
  }
  k2_need(lds, 2);
#define EXP4(S, b) do { _Pragma("unroll") for (int i = 0; i < 4; ++i) e[i] = cexp2(S[b + i]); PIN(e); SB(); } while (0)
#define EXP4P(P, S, b) do { P[0] = pk2(e[0], e[1]); P[1] = pk2(e[2], e[3]); \
    P[2] = pk2(cexp2(S[b]), cexp2(S[b + 1])); P[3] = pk2(cexp2(S[b + 2]), cexp2(S[b + 3])); PIN(P); SB(); } while (0)
#define THEAD(j, SC, SN, KN, KL, VL0, VL1) do { \
    EXP4(SC, 0); \
    SN = MFMA(KN, qf, negm); PIN(SN); SB(); \
    KL = lds_read16(kp + (j + 2) * 1024); VL0 = lds_read16(vp + (j + 1) * 1024); VL1 = lds_read16(vp + (j + 1) * 1024 + 512); SB(); \
    EXP4P(p0, SC, 4); } while (0)
#define TTAIL(SC, VU0, VU1) do { \
    o = MFMA(VU0, BC(p0), o); PIN(o); SB(); \
    EXP4(SC, 8); \
    EXP4P(p1, SC, 12); \
    o = MFMA(VU1, BC(p1), o); PIN(o); SB(); } while (0)
  f32x4 e;
  int it = 0;
#pragma unroll 1
  for (;;) {
    lds_cptr kp = kp0 + it * 4096, vp = vp0 + it * 4 * vinc;
    bf16x8 kA = lds_read16(kp + 1024), kB;
    bf16x8 vA0 = lds_read16(vp), vA1 = lds_read16(vp + 512), vB0, vB1;
    f32x16 negm = splat16(nm); PIN(negm);
    f32x16 sA = MFMA(lds_read16(kp), qf, negm), sB;
    u32x4 p0, p1;
    THEAD(0, sA, sB, kA, kB, vB0, vB1);
    bool trig;
#pragma unroll 1
    do {
      TTAIL(sA, vA0, vA1);
      THEAD(1, sB, sA, kB, kA, vA0, vA1); TTAIL(sB, vB0, vB1);
      THEAD(2, sA, sB, kA, kB, vB0, vB1); TTAIL(sA, vA0, vA1);
      THEAD(3, sB, sA, kB, kA, vA0, vA1); TTAIL(sB, vB0, vB1);
      kp += 4096; vp += 4 * vinc; ++it;
      if ((it & 1) == 0 && it < 14) k2_need(lds, (it >> 1) + 2);
      THEAD(0, sA, sB, kA, kB, vB0, vB1);
      trig = __any(o[8] >= 1.f);
    } while (it < K2_ITERS && !trig);
    if (__builtin_expect(!trig, 1)) break;
    k2_fix(o, nm, qf, kp0, vp0, vinc, 4 * it - 4, h);
    if (it >= K2_ITERS) break;
  }
  asm volatile("s_nop 15" : "+v"(o));
  const float il = 1.f / swap_add(h == 0 ? o[8] : 0.f);
  u32x4 ov = {pk2(mul_s(o[0], il), mul_s(o[1], il)), pk2(mul_s(o[2], il), mul_s(o[3], il)), pk2(mul_s(o[4], il), mul_s(o[5], il)), pk2(mul_s(o[6], il), mul_s(o[7], il))};
  st16_dev(ws + WS_A + ((size_t)mt * KST3 + head) * 1024 + lane * 16, ov);
}

DI void k2_ffn(char* __restrict__ ws) {
  const int lane = threadIdx.x & 63;
  const int fw = __builtin_amdgcn_readfirstlane(threadIdx.x >> 6) - 8;
  const int bid = blockIdx.x;
  const int sp = (bid & 7) + 8 * fw, mp = bid >> 3;
  if (sp < 43) {
    const __amdgpu_buffer_rsrc_t rs = __builtin_amdgcn_make_buffer_rsrc(ws, 0, 0x7fffffff, 0x00020000);
    const int xo = (int)WS_X + mp * 65536, wo = (int)WS_G + sp * 65536, l16 = lane * 16;
    f32x16 acc[2][2] = {{f32x16{}, f32x16{}}, {f32x16{}, f32x16{}}};
    constexpr int PF = 3;
    u32x4 a0[PF], a1[PF], b0[PF], b1[PF];
#define BLD(off) __builtin_amdgcn_raw_buffer_load_b128(rs, l16, (off), 0)
#define FF_LOAD(i, k) do { a0[i] = BLD(xo + (k) * 1024); a1[i] = BLD(xo + 32768 + (k) * 1024); \
    b0[i] = BLD(wo + (k) * 1024); b1[i] = BLD(wo + 32768 + (k) * 1024); } while (0)
#pragma unroll
    for (int i = 0; i < PF; ++i) FF_LOAD(i, i);
#pragma unroll
    for (int k = 0; k < 32; ++k) {
      const int i = k % PF;
      acc[0][0] = MFMA(BC(b0[i]), BC(a0[i]), acc[0][0]); acc[0][1] = MFMA(BC(b1[i]), BC(a0[i]), acc[0][1]);
      acc[1][0] = MFMA(BC(b0[i]), BC(a1[i]), acc[1][0]); acc[1][1] = MFMA(BC(b1[i]), BC(a1[i]), acc[1][1]);
      PIN(acc[1][1]); SB();
      if (k + PF < 32) FF_LOAD(i, k + PF);
      SB();
    }
    asm volatile("s_nop 15\n\ts_nop 15" : "+v"(acc[0][0]), "+v"(acc[0][1]), "+v"(acc[1][0]), "+v"(acc[1][1]));
    SB();
#pragma unroll
    for (int mi = 0; mi < 2; ++mi)
#pragma unroll
      for (int s = 0; s < 2; ++s) {
        float v[8];
#pragma unroll
        for (int j = 0; j < 8; ++j) {
          const float g = acc[mi][s][j], u = acc[mi][s][8 + j];
          float d = __builtin_amdgcn_exp2f(mul_s(g, -1.4426950408889634f)); add_s(d, 1.f);
          v[j] = mul_s(mul_s(g, __builtin_amdgcn_rcpf(d)), u);
        }
        u32x4 o = {pk2(v[0], v[1]), pk2(v[2], v[3]), pk2(v[4], v[5]), pk2(v[6], v[7])};
        st16_dev(ws + WS_A + ((size_t)(mp * 2 + mi) * KST3 + 32 + sp * 2 + s) * 1024 + lane * 16, o);
      }
  }
}

constexpr int K3_ROWB = 64 * 4 + 16;
__global__ __launch_bounds__(1024, 4) void k2_kernel(char* __restrict__ ws, const u16* __restrict__ wo, const u16* __restrict__ wd,
                                                     const u16* __restrict__ x, u16* __restrict__ out) {
  __shared__ __attribute__((aligned(16))) char lds[K2_LDS];
  if (threadIdx.x < 32) *(unsigned*)(lds + K2_CNT + threadIdx.x * 4) = 0u;
  __syncthreads();
  const int wv = __builtin_amdgcn_readfirstlane(threadIdx.x >> 6);
  if (wv < 8) k2_attn(lds, ws); else if (wv < 14) k2_ffn(ws); else k2_loader(lds, ws, wv - 14, wo, wd);
  __builtin_amdgcn_s_setprio(0);
  const int tid = threadIdx.x, lane = tid & 63, r = lane & 31, h = lane >> 5, bid = blockIdx.x;
  WAITV(0);
  __syncthreads();
  if (tid == 0) {
    unsigned* cg = (unsigned*)(ws + WS_C + (bid >> 5) * 256);
    unsigned* cw = (unsigned*)(ws + WS_C + 8 * 256);
    __hip_atomic_fetch_add(cg, 1u, __ATOMIC_RELAXED, __HIP_MEMORY_SCOPE_AGENT);
    for (int spin = 0; spin < (1 << 22); ++spin) {
      if (__hip_atomic_load(cg, __ATOMIC_RELAXED, __HIP_MEMORY_SCOPE_AGENT) >= 32u &&
          __hip_atomic_load(cw, __ATOMIC_RELAXED, __HIP_MEMORY_SCOPE_AGENT) >= 512u) break;
      __builtin_amdgcn_s_sleep(2);
    }
  }
  __syncthreads();
  const int mb = (bid >> 5) * 4 + (bid & 3), nb = (bid >> 2) & 7;
  if (wv < 8) {
    const int k0 = wv * 15 - (wv > 6 ? 1 : 0), nk = (wv < 6) ? 15 : 14;
    const __amdgpu_buffer_rsrc_t rs = __builtin_amdgcn_make_buffer_rsrc(ws, 0, 0x7fffffff, 0x00020000);
    const int ao = (int)WS_A + (mb * 2 * KST3 + k0) * 1024, wo_ = (int)WS_W + (k0 * 16 + nb * 2) * 1024, l16 = lane * 16;
    f32x16 acc[2][2] = {{f32x16{}, f32x16{}}, {f32x16{}, f32x16{}}};
    constexpr int PF = 3;
    u32x4 a0[PF], a1[PF], b0[PF], b1[PF];
    const int klast = nk - 1;
#define BLD1(off) __builtin_amdgcn_raw_buffer_load_b128(rs, l16, (off), 16)
#define K3_LOAD(i, k) do { const int kk_ = ((k) < 14) ? (k) : klast; \
    a0[i] = BLD1(ao + kk_ * 1024); a1[i] = BLD1(ao + KST3 * 1024 + kk_ * 1024); \
    b0[i] = BLD1(wo_ + kk_ * 16384); b1[i] = BLD1(wo_ + kk_ * 16384 + 1024); } while (0)
#pragma unroll
    for (int i = 0; i < PF; ++i) K3_LOAD(i, i);
#pragma unroll
    for (int k = 0; k < 15; ++k) {
      const int i = k % PF;
      if (k < 14 || nk == 15) {
        acc[0][0] = MFMA(BC(b0[i]), BC(a0[i]), acc[0][0]); acc[0][1] = MFMA(BC(b1[i]), BC(a0[i]), acc[0][1]);
        acc[1][0] = MFMA(BC(b0[i]), BC(a1[i]), acc[1][0]); acc[1][1] = MFMA(BC(b1[i]), BC(a1[i]), acc[1][1]);
      }
      PIN(acc[1][1]); SB();
      if (k + PF < 15) K3_LOAD(i, k + PF);
      SB();
    }
    char* pw = lds + wv * 64 * K3_ROWB;
#pragma unroll
    for (int i = 0; i < 2; ++i)
#pragma unroll
      for (int j = 0; j < 2; ++j)
#pragma unroll
        for (int g = 0; g < 4; ++g) {
          f32x4 v = {acc[i][j][4 * g], acc[i][j][4 * g + 1], acc[i][j][4 * g + 2], acc[i][j][4 * g + 3]};
          *(f32x4*)(pw + (i * 32 + r) * K3_ROWB + (j * 32 + 8 * g + 4 * h) * 4) = v;
        }
  }
  const int m = tid >> 4, n0 = (tid & 15) * 4;
  const size_t go = (size_t)(mb * 64 + m) * D_ + nb * 64 + n0;
  const u32x2 xv = *(const u32x2*)(x + go);
  __syncthreads();
  f32x4 s = {lo16(xv[0]), hi16(xv[0]), lo16(xv[1]), hi16(xv[1])};
#pragma unroll
  for (int w = 0; w < 8; ++w) s += *(const f32x4*)(lds + w * 64 * K3_ROWB + m * K3_ROWB + n0 * 4);
  u32x2 o = {pk2(s[0], s[1]), pk2(s[2], s[3])};
  *(u32x2*)(out + go) = o;
}

extern "C" void kernel_launch(void* const* d_in, const int* in_sizes, int n_in, void* d_out, int out_size, void* d_ws, size_t ws_size,
                              hipStream_t stream) {
  const u16* x = (const u16*)d_in[0];
  const u16* wqkv = (const u16*)d_in[1];
  const u16* wo = (const u16*)d_in[2];
  const u16* wg = (const u16*)d_in[4];
  const u16* wu = (const u16*)d_in[5];
  const u16* wd = (const u16*)d_in[6];
  char* ws = (char*)d_ws;
  k1_kernel<<<256, 768, 0, stream>>>(x, wqkv, wg, wu, ws);
  k2_kernel<<<256, 1024, 0, stream>>>(ws, wo, wd, x, (u16*)d_out);
}
```
